```python
import torch

def _softmax(x, axis=-1):
    return torch.softmax(x, dim=axis)
def _gelu(x):
    return torch.nn.functional.gelu(x, approximate="tanh")
def _silu(x):
    return torch.nn.functional.silu(x)
def _sigmoid(x):
    return torch.sigmoid(x)
def _layernorm(x, axis=-1, eps=1e-5):
    m = x.mean(dim=axis, keepdim=True)
    v = x.var(dim=axis, keepdim=True, unbiased=False)
    return (x - m) / torch.sqrt(v + eps)
def _scatter_add(tgt, idx, src, axis=0):
    return tgt.clone().index_add_(axis, idx, src)
def _segment_sum(src, seg, n):
    out = torch.zeros((n,) + src.shape[1:], dtype=src.dtype)
    return out.index_add_(0, seg, src)
def _pad(x, pw):
    flat = tuple(v for pair in reversed(pw) for v in pair)
    return torch.nn.functional.pad(x, flat)

kernel_name = 'standalone__softmax_full_bidir__decode'

def setup_inputs():

    _w = (lambda a: (lambda t: t.to(torch.bfloat16) if t.is_floating_point() else t)(torch.as_tensor(a)))
    B, H, Sq, Skv, Dh = 4, 16, 1, 16384, 256
    return {
        "q": _w((lambda *s: torch.randn(*s, dtype=torch.bfloat16) * 0.8)(B, H, Sq, Dh)),
        "k": _w((lambda *s: torch.randn(*s, dtype=torch.bfloat16) * 0.8)(B, H, Skv, Dh)),
        "v": _w((lambda *s: torch.randn(*s, dtype=torch.bfloat16) * 0.05)(B, H, Skv, Dh)),
    }

def reference(q, k, v):
    scale = q.shape[-1] ** -0.5
    scores = torch.einsum('bhqd,bhkd->bhqk', q, k) * scale
    attn = _softmax(scores, axis=-1)
    return torch.einsum('bhqk,bhkd->bhqd', attn, v)
```

```cpp
#include <hip/hip_runtime.h>
#include <hip/hip_bf16.h>
#include <stdint.h>

#define NSPLIT 8
#define WAVES 4

constexpr int BH = 64, SKV = 16384, D = 256;
constexpr int NL = 8;
constexpr int TK = 2 * NL;
constexpr int KEYS_PER_SPLIT = SKV / NSPLIT;
constexpr int TILES_PER_WAVE = KEYS_PER_SPLIT / (TK * WAVES);
constexpr float C2 = 0.0625f * 1.4426950408889634f;

using u32x4 = __attribute__((ext_vector_type(4))) unsigned;
using f32x2 = __attribute__((ext_vector_type(2))) float;
using f32x4 = __attribute__((ext_vector_type(4))) float;
using bf16x2 = __attribute__((ext_vector_type(2))) __bf16;

__device__ __forceinline__ float dot2(unsigned k, unsigned q, float acc) {
  return __builtin_amdgcn_fdot2_f32_bf16(__builtin_bit_cast(bf16x2, k), __builtin_bit_cast(bf16x2, q), acc, false);
}
template <int CTRL> __device__ __forceinline__ float dpp(float x) {
  return __builtin_bit_cast(float, __builtin_amdgcn_mov_dpp(__builtin_bit_cast(int, x), CTRL, 0xf, 0xf, true));
}
constexpr int XOR1 = 0xB1, XOR2 = 0x4E, XOR7 = 0x141, XOR8 = 0x128;
constexpr int NEWBCAST = 0x150;

__device__ __forceinline__ float swap16_sum(float x) {
  auto s = __builtin_amdgcn_permlane16_swap(__float_as_uint(x), __float_as_uint(x), false, false);
  return __uint_as_float(s[0]) + __uint_as_float(s[1]);
}

struct Tile { u32x4 r[NL]; };

__device__ __forceinline__ void issue(Tile& t, const u32x4* p) {
#pragma unroll
  for (int i = 0; i < NL; ++i) t.r[i] = __builtin_nontemporal_load(p + i * 64);
}

template <int I> struct Bc { static __device__ __forceinline__ float get(float p) { return dpp<NEWBCAST + 2 * I>(p); } };

__device__ __forceinline__ void step(const Tile& K, const Tile& V, const u32x4 q, int c, float& m, float& l, f32x2 (&acc)[4]) {
  float x[NL];
#pragma unroll
  for (int i = 0; i < NL; ++i) {
    float d = 0.f;
#pragma unroll
    for (int j = 0; j < 4; ++j) d = dot2(K.r[i][j], q[j], d);
    x[i] = d;
  }
#define TR_STEP(HALF, CTRL, BIT)                                            \
  _Pragma("unroll") for (int h = 0; h < (HALF); ++h) {                      \
    const float lo = x[h], hi = x[h + (HALF)];                              \
    const float t1 = lo + dpp<CTRL>(lo), t2 = hi + dpp<CTRL>(hi);           \
    x[h] = (c & (BIT)) ? t2 : t1;                                           \
  }
  TR_STEP(4, XOR8, 8) TR_STEP(2, XOR7, 4) TR_STEP(1, XOR2, 2)
#undef TR_STEP
  float s = x[0] + dpp<XOR1>(x[0]);
  s = swap16_sum(s) * C2;
  float pm = fmaxf(s, dpp<XOR2>(s));
  pm = fmaxf(pm, dpp<XOR7>(pm));
  pm = fmaxf(pm, dpp<XOR8>(pm));
  const float mn = fmaxf(m, pm);
  const float alpha = __builtin_amdgcn_exp2f(m - mn);
  const float p = __builtin_amdgcn_exp2f(s - mn);
  l = fmaf(l, alpha, p);
  m = mn;
#pragma unroll
  for (int j = 0; j < 4; ++j) acc[j] *= alpha;
  float pb[NL] = {Bc<0>::get(p), Bc<1>::get(p), Bc<2>::get(p), Bc<3>::get(p), Bc<4>::get(p), Bc<5>::get(p), Bc<6>::get(p), Bc<7>::get(p)};
#pragma unroll
  for (int i = 0; i < NL; ++i) {
    const f32x2 pp = {pb[i], pb[i]};
#pragma unroll
    for (int j = 0; j < 4; ++j) {
      const unsigned w = V.r[i][j];
      const f32x2 vv = {__uint_as_float(w << 16), __uint_as_float(w & 0xffff0000u)};
      acc[j] = __builtin_elementwise_fma(pp, vv, acc[j]);
    }
  }
}

__global__ void __launch_bounds__(WAVES * 64) attn_split(const u32x4* __restrict__ q, const u32x4* __restrict__ k,
                                                          const u32x4* __restrict__ v, float* __restrict__ ws) {
  __shared__ __attribute__((aligned(16))) float lds[WAVES * 2 * (D + 4)];
  const int bh = blockIdx.x / NSPLIT, sp = blockIdx.x % NSPLIT;
  const int tid = threadIdx.x, lane = tid & 63, wave = __builtin_amdgcn_readfirstlane(tid >> 6);
  const int c = lane & 15;
  const size_t base = ((size_t)bh * SKV + (size_t)sp * KEYS_PER_SPLIT + (size_t)wave * TK) * 32 + lane;
  const u32x4* kp = k + base;
  const u32x4* vp = v + base;
  constexpr int STRIDE = WAVES * TK * 32;
  Tile Ka, Va, Kb, Vb;
  issue(Ka, kp); issue(Va, vp);
  const u32x4 qv = q[bh * 32 + (lane & 31)];
  float m = -INFINITY, l = 0.f;
  f32x2 acc[4] = {{0.f, 0.f}, {0.f, 0.f}, {0.f, 0.f}, {0.f, 0.f}};
  static_assert(TILES_PER_WAVE % 2 == 0, "");
  for (int t = 0; t < TILES_PER_WAVE - 2; t += 2) {
    issue(Kb, kp + STRIDE); issue(Vb, vp + STRIDE);
    step(Ka, Va, qv, c, m, l, acc);
    kp += 2 * STRIDE; vp += 2 * STRIDE;
    issue(Ka, kp); issue(Va, vp);
    step(Kb, Vb, qv, c, m, l, acc);
  }
  issue(Kb, kp + STRIDE); issue(Vb, vp + STRIDE);
  step(Ka, Va, qv, c, m, l, acc);
  step(Kb, Vb, qv, c, m, l, acc);
  l += dpp<XOR2>(l); l += dpp<XOR7>(l); l += dpp<XOR8>(l);
  {
    float* rec = lds + (wave * 2 + (lane >> 5)) * (D + 4);
    const int cc = lane & 31;
    *reinterpret_cast<f32x4*>(rec + 4 + 8 * cc) = f32x4{acc[0][0], acc[0][1], acc[1][0], acc[1][1]};
    *reinterpret_cast<f32x4*>(rec + 8 + 8 * cc) = f32x4{acc[2][0], acc[2][1], acc[3][0], acc[3][1]};
    if (cc == 0) { rec[0] = m; rec[1] = l; }
  }
  __syncthreads();
  if (tid < D) {
    float M = -INFINITY;
#pragma unroll
    for (int w = 0; w < WAVES * 2; ++w) M = fmaxf(M, lds[w * (D + 4)]);
    float L = 0.f, o = 0.f;
#pragma unroll
    for (int w = 0; w < WAVES * 2; ++w) {
      const float* rec = lds + w * (D + 4);
      const float wt = __builtin_amdgcn_exp2f(rec[0] - M);
      L = fmaf(wt, rec[1], L);
      o = fmaf(wt, rec[4 + tid], o);
    }
    float* po = ws + (size_t)blockIdx.x * D;
    float* pml = ws + (size_t)BH * NSPLIT * D + (size_t)blockIdx.x * 2;
    po[tid] = o;
    if (tid == 0) { pml[0] = M; pml[1] = L; }
  }
}

__global__ void __launch_bounds__(D) attn_combine(const float* __restrict__ ws, __hip_bfloat16* __restrict__ out) {
  const int bh = blockIdx.x, d = threadIdx.x;
  const float* po = ws + (size_t)bh * NSPLIT * D;
  const float* pml = ws + (size_t)BH * NSPLIT * D + (size_t)bh * NSPLIT * 2;
  float ms[NSPLIT], ls[NSPLIT], os[NSPLIT];
#pragma unroll
  for (int s = 0; s < NSPLIT; ++s) { ms[s] = pml[2 * s]; ls[s] = pml[2 * s + 1]; os[s] = po[s * D + d]; }
  float M = -INFINITY;
#pragma unroll
  for (int s = 0; s < NSPLIT; ++s) M = fmaxf(M, ms[s]);
  float L = 0.f, o = 0.f;
#pragma unroll
  for (int s = 0; s < NSPLIT; ++s) {
    const float wt = __builtin_amdgcn_exp2f(ms[s] - M);
    L = fmaf(wt, ls[s], L);
    o = fmaf(wt, os[s], o);
  }
  out[bh * D + d] = __float2bfloat16(o / L);
}

extern "C" void kernel_launch(void* const* d_in, const int* in_sizes, int n_in, void* d_out, int out_size, void* d_ws,
                              size_t ws_size, hipStream_t stream) {
  const u32x4* q = (const u32x4*)d_in[0];
  const u32x4* k = (const u32x4*)d_in[1];
  const u32x4* v = (const u32x4*)d_in[2];
  attn_split<<<BH * NSPLIT, WAVES * 64, 0, stream>>>(q, k, v, (float*)d_ws);
  attn_combine<<<BH, D, 0, stream>>>((const float*)d_ws, (__hip_bfloat16*)d_out);
}
```

```cpp
#include <hip/hip_runtime.h>
#include <hip/hip_bf16.h>
#include <stdint.h>
#include <type_traits>

constexpr int BH = 64, SKV = 16384, D = 256;
constexpr int NSPLIT = 4, WAVES = 4;
constexpr unsigned TENSOR_BYTES = BH * SKV * D * 2, REG = SKV * D * 2 / NSPLIT;
constexpr int RING = 8;
constexpr unsigned ROW = WAVES * 1024;
constexpr int NIT = REG / ROW / RING;
constexpr float C2 = 0.0625f * 1.4426950408889634f;
constexpr float NEG_BIG = -3.4028234663852886e38f;
constexpr int T_ALL = 13950, T_FIRST = 100, LEVEL0 = 11, LEVEL_MAX = 24, GAIN_K = 8, GAIN_V = 12;
constexpr unsigned T_NOTE = 7200, T_CHANGE = 8000, T_LOOK = 8400;
constexpr int MB = 288, WS_A = 3 * MB, WS_HEAD = WS_A + 32;
constexpr unsigned SPIN_CAP = 1u << 18;
constexpr int AUX_NT = 2, AUX_SC1 = 16;
constexpr int REC = D + 4, LDS_MRG = WAVES * NIT * RING * 2, LDS_OLD = LDS_MRG + WAVES * REC, LDS_FLOATS = LDS_OLD + 3 * MB + 4;

using u32x2 = __attribute__((ext_vector_type(2))) unsigned;
using u32x4 = __attribute__((ext_vector_type(4))) unsigned;
using f32x2 = __attribute__((ext_vector_type(2))) float;
using f32x4 = __attribute__((ext_vector_type(4))) float;
using bf16x2 = __attribute__((ext_vector_type(2))) __bf16;
using bf16x4 = __attribute__((ext_vector_type(4))) __bf16;
using rsrc_t = __amdgpu_buffer_rsrc_t;

__device__ __forceinline__ rsrc_t rsrc(const void* p, unsigned bytes) {
  return __builtin_amdgcn_make_buffer_rsrc((void*)p, 0, (int)bytes, 0x00020000);
}
__device__ __forceinline__ float dot2(unsigned k, unsigned q, float acc) {
  return __builtin_amdgcn_fdot2_f32_bf16(__builtin_bit_cast(bf16x2, k), __builtin_bit_cast(bf16x2, q), acc, false);
}
template <int CTRL> __device__ __forceinline__ float dpp(float x) {
  return __builtin_bit_cast(float, __builtin_amdgcn_mov_dpp(__builtin_bit_cast(int, x), CTRL, 0xf, 0xf, true));
}
constexpr int XOR1 = 0xB1, XOR2 = 0x4E, XOR7 = 0x141, XOR8 = 0x128;
__device__ __forceinline__ f32x2 swap16(float x) {
  auto s = __builtin_amdgcn_permlane16_swap(__float_as_uint(x), __float_as_uint(x), false, false);
  return f32x2{__uint_as_float(s[0]), __uint_as_float(s[1])};
}
__device__ __forceinline__ f32x2 swap32(float x, float y) {
  auto s = __builtin_amdgcn_permlane32_swap(__float_as_uint(x), __float_as_uint(y), false, false);
  return f32x2{__uint_as_float(s[0]), __uint_as_float(s[1])};
}
__device__ __forceinline__ float wave_max(float x) {
  x = fmaxf(x, dpp<XOR1>(x)); x = fmaxf(x, dpp<XOR2>(x)); x = fmaxf(x, dpp<XOR7>(x)); x = fmaxf(x, dpp<XOR8>(x));
  f32x2 s = swap16(x);
  x = fmaxf(s[0], s[1]);
  s = swap32(x, x);
  return fmaxf(s[0], s[1]);
}
__device__ __forceinline__ void sleep_until(unsigned long long t0, unsigned ticks) {
  while ((unsigned)(__builtin_amdgcn_s_memrealtime() - t0) < ticks) __builtin_amdgcn_s_sleep(32);
}
__device__ __forceinline__ void fence() { asm volatile("" ::: "memory"); __builtin_amdgcn_sched_barrier(0); }
template <int I, int N, typename F> __device__ __forceinline__ void static_for(F&& f) {
  if constexpr (I < N) { f(std::integral_constant<int, I>{}); static_for<I + 1, N>(f); }
}

template <int J> __device__ __forceinline__ void throttle(int level) {
  constexpr int JR = ((J & 1) << 2) | (J & 2) | ((J >> 2) & 1);
  asm volatile("s_waitcnt vmcnt(6)\n\ts_cmp_gt_i32 %0, %1\n\ts_cbranch_scc1 1f\n\t"
               "s_waitcnt vmcnt(5)\n\ts_cmp_gt_i32 %0, %2\n\ts_cbranch_scc1 1f\n\t"
               "s_waitcnt vmcnt(4)\n\ts_cmp_gt_i32 %0, %3\n\ts_cbranch_scc1 1f\n\t"
               "s_waitcnt vmcnt(3)\n1:" ::"s"(level), "n"(16 + JR), "n"(8 + JR), "n"(JR) : "scc");
}
template <int GAIN> __device__ __forceinline__ int level_at(unsigned long long now, unsigned tstart, int it) {
  asm volatile("" : "+s"(now));
  const int lag = (int)((unsigned)now - tstart) - T_FIRST - ((it * T_ALL) >> 7);
  return LEVEL0 + ((lag * GAIN) >> 8);
}

__device__ __forceinline__ float reduce4(const float (&x)[4], int c) {
  const float a0 = x[0] + dpp<XOR8>(x[0]), a2 = x[2] + dpp<XOR8>(x[2]), a1 = x[1] + dpp<XOR8>(x[1]), a3 = x[3] + dpp<XOR8>(x[3]);
  const float y0 = (c & 8) ? a2 : a0, y1 = (c & 8) ? a3 : a1;
  const float b0 = y0 + dpp<XOR7>(y0), b1 = y1 + dpp<XOR7>(y1);
  float s = (c & 4) ? b1 : b0;
  s += dpp<XOR2>(s);
  s += dpp<XOR1>(s);
  const f32x2 t = swap16(s);
  return (t[0] + t[1]) * C2;
}

__global__ void __launch_bounds__((WAVES + 1) * 64, 2)
attn_decode(const u32x4* __restrict__ q, const void* k, const void* v, unsigned* ws, bf16x4* __restrict__ out) {
  __shared__ __attribute__((aligned(16))) float lds[LDS_FLOATS];
  unsigned long long t0 = __builtin_amdgcn_s_memrealtime();
  asm volatile("" ::"s"(q), "s"(k), "s"(v), "s"(ws), "s"(out));
  const int bh = blockIdx.x / NSPLIT, member = blockIdx.x % NSPLIT;
  const int lane = threadIdx.x & 63, wave = __builtin_amdgcn_readfirstlane(threadIdx.x >> 6);
  unsigned* old = reinterpret_cast<unsigned*>(lds + LDS_OLD);
  const rsrc_t rw = rsrc(ws + bh * WS_HEAD, WS_HEAD * 4);

  if (wave == WAVES) {
    __builtin_amdgcn_s_setprio(3);
    sleep_until(t0, T_NOTE);
    const unsigned a0 = __builtin_amdgcn_readfirstlane(__builtin_amdgcn_raw_buffer_load_b32(rw, WS_A * 4, 0, AUX_SC1 | AUX_NT));
    const int j0 = member == 0 ? 0 : member - 1, j1 = member == 0 ? 3 : member;
    for (int j = j0; j < j1; ++j) {
      const u32x4 o4 = __builtin_amdgcn_raw_buffer_load_b128(rw, lane * 16, j * MB * 4, AUX_SC1 | AUX_NT);
      const u32x2 ml = __builtin_amdgcn_raw_buffer_load_b64(rw, D * 4, j * MB * 4, AUX_SC1 | AUX_NT);
      *reinterpret_cast<u32x4*>(old + j * MB + lane * 4) = o4;
      *reinterpret_cast<u32x2*>(old + j * MB + D) = ml;
    }
    if (member == 0) {
      old[3 * MB] = a0 + 1u;
      asm volatile("s_waitcnt vmcnt(0) lgkmcnt(0)" ::: "memory");
      sleep_until(t0, T_CHANGE);
      if (lane == 0) __builtin_amdgcn_raw_buffer_store_b32(a0 + 1u, rw, WS_A * 4, 0, AUX_SC1);
    } else {
      sleep_until(t0, T_LOOK);
#pragma unroll 1
      for (unsigned spins = 0; spins < SPIN_CAP; ++spins) {
        asm volatile("" ::: "memory");
        if ((unsigned)__builtin_amdgcn_readfirstlane(__builtin_amdgcn_raw_buffer_load_b32(rw, WS_A * 4, 0, AUX_SC1 | AUX_NT)) != a0) break;
        __builtin_amdgcn_s_sleep(64);
      }
    }
    asm volatile("s_waitcnt vmcnt(0) lgkmcnt(0)" ::: "memory");
    return;
  }

  const unsigned voff = lane * 16, base = blockIdx.x * REG + wave * 1024;
  const u32x4 qv = __builtin_nontemporal_load(q + bh * 32 + (lane & 31));
  u32x4 T[RING];
  static_for<0, RING>([&](auto j) { T[j] = __builtin_amdgcn_raw_buffer_load_b128(rsrc(k, TENSOR_BYTES), voff, base + j * ROW, AUX_NT); });
  fence();
  asm volatile("" : "+s"(t0));
  const unsigned tstart = (unsigned)t0;
  const int c = lane & 15;
  float* sc = lds + wave * NIT * RING * 2;
  int level = 16;

  float m = NEG_BIG;
  {
    float* w = sc + (lane >> 5) * 4 + (c >> 2);
#pragma unroll 1
    for (int it = 0; it < NIT; ++it, w += RING * 2) {
      const unsigned long long now = __builtin_amdgcn_s_memrealtime();
      const bool last = it == NIT - 1;
      const rsrc_t r = rsrc(last ? v : k, TENSOR_BYTES);
      const unsigned s = last ? base : base + (it + 1) * RING * ROW;
      level = last ? LEVEL_MAX : level;
      float x[4];
      fence();
      static_for<0, RING>([&](auto jc) {
        constexpr int J = jc;
        float d = 0.f;
#pragma unroll
        for (int i = 0; i < 4; ++i) d = dot2(T[J][i], qv[i], d);
        x[J % 4] = d;
        asm volatile("" : "+v"(x[J % 4]));
        throttle<J>(level);
        T[J] = __builtin_amdgcn_raw_buffer_load_b128(r, voff, s + J * ROW, AUX_NT);
        fence();
        if constexpr (J % 4 == 3) {
          const float sv = reduce4(x, c);
          m = fmaxf(m, sv);
          w[(J / 4) * 8] = sv;
          fence();
        }
      });
      level = level_at<GAIN_K>(now, tstart, it);
    }
  }
  m = wave_max(m);
  fence();

  float l = 0.f;
  f32x2 acc[4] = {{0.f, 0.f}, {0.f, 0.f}, {0.f, 0.f}, {0.f, 0.f}};
  {
    const f32x4* pr = reinterpret_cast<const f32x4*>(sc) + (lane >> 5);
    f32x4 s0 = pr[0], s1;
#pragma unroll 1
    for (int it = 0; it < NIT; ++it) {
      unsigned long long now;
      const bool last = it == NIT - 1;
      const rsrc_t r = rsrc(v, last ? 0u : TENSOR_BYTES);
      const unsigned s = base + (it + 1) * RING * ROW;
      level = last ? LEVEL_MAX : level;
      fence();
      static_for<0, RING>([&](auto jc) {
        constexpr int J = jc;
        if constexpr (J == 1) { now = __builtin_amdgcn_s_memrealtime(); s1 = pr[2]; }
        if constexpr (J == 4) { pr += 4; s0 = pr[0]; }
        const float p = __builtin_amdgcn_exp2f((J < 4 ? s0[J % 4] : s1[J % 4]) - m);
        l += p;
        const f32x2 pp = {p, p};
#pragma unroll
        for (int i = 0; i < 4; ++i) {
          const unsigned w = T[J][i];
          acc[i] = __builtin_elementwise_fma(pp, f32x2{__uint_as_float(w << 16), __uint_as_float(w & 0xffff0000u)}, acc[i]);
        }
        asm volatile("" : "+v"(acc[0]), "+v"(acc[1]), "+v"(acc[2]), "+v"(acc[3]));
        throttle<J>(level);
        T[J] = __builtin_amdgcn_raw_buffer_load_b128(r, voff, s + J * ROW, AUX_NT);
        fence();
      });
      level = level_at<GAIN_V>(now, tstart, NIT + it);
    }
  }

  float* mrg = lds + LDS_MRG;
  {
    f32x4 h;
#pragma unroll
    for (int i = 0; i < 4; ++i) {
      const f32x2 t = swap32(acc[i / 2][i % 2], acc[2 + i / 2][i % 2]);
      h[i] = t[0] + t[1];
    }
    const f32x2 l2 = swap32(l, l);
    float* rec = mrg + wave * REC;
    *reinterpret_cast<f32x4*>(rec + 4 + 8 * (lane & 31) + 4 * (lane >> 5)) = h;
    *reinterpret_cast<f32x2*>(rec) = f32x2{m, l2[0] + l2[1]};
  }
  asm volatile("s_waitcnt lgkmcnt(0)" ::: "memory");
  __builtin_amdgcn_s_barrier();
  asm volatile("" ::: "memory");
  if (wave != 0) return;
  f32x2 ml[WAVES];
  f32x4 ow[WAVES];
  u32x4 so[3];
  u32x2 sm[3];
#pragma unroll
  for (int w = 0; w < WAVES; ++w) {
    ml[w] = *reinterpret_cast<const f32x2*>(mrg + w * REC);
    ow[w] = *reinterpret_cast<const f32x4*>(mrg + w * REC + 4 + 4 * lane);
  }
#pragma unroll
  for (int j = 0; j < 3; ++j) {
    const unsigned* od = old + (j == 0 && member != 0 ? member - 1 : j) * MB;
    so[j] = *reinterpret_cast<const u32x4*>(od + 4 * lane);
    sm[j] = *reinterpret_cast<const u32x2*>(od + D);
  }
  unsigned a = old[3 * MB];
  fence();
  float M = fmaxf(fmaxf(ml[0][0], ml[1][0]), fmaxf(ml[2][0], ml[3][0]));
  M = __uint_as_float(__float_as_uint(M) & ~1u);
  float L = 0.f;
  f32x4 o = {0.f, 0.f, 0.f, 0.f};
#pragma unroll
  for (int w = 0; w < WAVES; ++w) {
    const float wt = __builtin_amdgcn_exp2f(ml[w][0] - M);
    L = fmaf(wt, ml[w][1], L);
    o += wt * ow[w];
  }
  if (member != 0) {
    const u32x4 wo = (__builtin_bit_cast(u32x4, o) & ~1u) | (~so[0] & 1u);
    const u32x2 wm = (u32x2{__float_as_uint(M), __float_as_uint(L)} & ~1u) | (~sm[0] & 1u);
    __builtin_amdgcn_raw_buffer_store_b128(wo, rw, lane * 16, (member - 1) * MB * 4, AUX_SC1);
    __builtin_amdgcn_raw_buffer_store_b64(wm, rw, D * 4 + lane * (WS_HEAD * 4), (member - 1) * MB * 4, AUX_SC1);
    return;
  }

  u32x4 uo[3];
  u32x2 um[3];
#pragma unroll 1
  for (unsigned spins = 1; spins < SPIN_CAP; ++spins) {
#pragma unroll
    for (int j = 0; j < 3; ++j) {
      uo[j] = __builtin_amdgcn_raw_buffer_load_b128(rw, lane * 16, j * MB * 4, AUX_SC1 | AUX_NT);
      um[j] = __builtin_amdgcn_raw_buffer_load_b64(rw, D * 4, j * MB * 4, AUX_SC1 | AUX_NT);
    }
    fence();
    unsigned ok = 1u;
#pragma unroll
    for (int j = 0; j < 3; ++j) {
      const u32x4 x4 = uo[j] ^ so[j];
      const u32x2 x2 = um[j] ^ sm[j];
      ok &= x4[0] & x4[1] & x4[2] & x4[3] & x2[0] & x2[1];
    }
    if (__all(ok & 1u)) break;
    if ((spins & 63u) == 0u && lane == 0)
      __builtin_amdgcn_raw_buffer_store_b32(++a, rw, WS_A * 4, 0, AUX_SC1);
  }
  float MM = M;
#pragma unroll
  for (int j = 0; j < 3; ++j) MM = fmaxf(MM, __uint_as_float(um[j][0] & ~1u));
  const float w0 = __builtin_amdgcn_exp2f(M - MM);
  L *= w0;
  o *= w0;
#pragma unroll
  for (int j = 0; j < 3; ++j) {
    const float wt = __builtin_amdgcn_exp2f(__uint_as_float(um[j][0] & ~1u) - MM);
    L = fmaf(wt, __uint_as_float(um[j][1] & ~1u), L);
    o += wt * __builtin_bit_cast(f32x4, uo[j] & ~1u);
  }
  out[bh * 64 + lane] = __builtin_convertvector(o * (1.f / L), bf16x4);
}

extern "C" void kernel_launch(void* const* d_in, const int* in_sizes, int n_in, void* d_out, int out_size, void* d_ws,
                              size_t ws_size, hipStream_t stream) {
  attn_decode<<<BH * NSPLIT, (WAVES + 1) * 64, 0, stream>>>((const u32x4*)d_in[0], d_in[1], d_in[2], (unsigned*)d_ws, (bf16x4*)d_out);
}
```

```cpp
#include <hip/hip_runtime.h>
#include <hip/hip_bf16.h>
#include <stdint.h>
#include <type_traits>

constexpr int BH = 64, SKV = 16384, D = 256;
constexpr int NSPLIT = 4, WAVES = 4;
constexpr unsigned HEAD_BYTES = SKV * D * 2, TENSOR_BYTES = BH * HEAD_BYTES;
constexpr int RING = 8;
constexpr int NIT = SKV / (NSPLIT * WAVES * RING * 2);
constexpr int BIT_HALF = 13, BIT_WAVE[2] = {16, 14}, BIT_LOAD[3] = {9, 15, 20}, BIT_IT[6] = {22, 21, 17, 19, 10, 12}, BIT_WG[2] = {18, 11};
template <int N> __host__ __device__ constexpr unsigned spread(unsigned x, const int (&bit)[N]) {
  unsigned r = 0;
  for (int b = 0; b < N; ++b) r |= ((x >> b) & 1u) << bit[b];
  return r;
}
static_assert((spread(1, {BIT_HALF}) | spread(3, BIT_WAVE) | spread(7, BIT_LOAD) | spread(63, BIT_IT) | spread(3, BIT_WG)) == HEAD_BYTES - D * 2, "");
constexpr float C2 = 0.0625f * 1.4426950408889634f;
constexpr float NEG_BIG = -3.4028234663852886e38f;
constexpr int T_ALL = 13680, T_FIRST = 100, LEVEL0 = 11, LEVEL_MAX = 24, GAIN_K = 8, GAIN_V = 12;
constexpr unsigned T_NOTE = 7200, T_CHANGE = 8000, T_LOOK = 8400;
constexpr int MB = 288, WS_A = 3 * MB, WS_HEAD = WS_A + 32;
constexpr unsigned SPIN_CAP = 1u << 18;
constexpr int AUX_NT = 2, AUX_SC1 = 16;
constexpr int REC = D + 4, LDS_MRG = WAVES * NIT * RING * 2, LDS_OLD = LDS_MRG + WAVES * REC, LDS_FLOATS = LDS_OLD + 3 * MB + 4;

using u32x2 = __attribute__((ext_vector_type(2))) unsigned;
using u32x4 = __attribute__((ext_vector_type(4))) unsigned;
using f32x2 = __attribute__((ext_vector_type(2))) float;
using f32x4 = __attribute__((ext_vector_type(4))) float;
using bf16x2 = __attribute__((ext_vector_type(2))) __bf16;
using bf16x4 = __attribute__((ext_vector_type(4))) __bf16;
using rsrc_t = __amdgpu_buffer_rsrc_t;

__device__ __forceinline__ rsrc_t rsrc(const void* p, unsigned bytes) {
  return __builtin_amdgcn_make_buffer_rsrc((void*)p, 0, (int)bytes, 0x00020000);
}
__device__ __forceinline__ float dot2(unsigned k, unsigned q, float acc) {
  return __builtin_amdgcn_fdot2_f32_bf16(__builtin_bit_cast(bf16x2, k), __builtin_bit_cast(bf16x2, q), acc, false);
}
template <int CTRL> __device__ __forceinline__ float dpp(float x) {
  return __builtin_bit_cast(float, __builtin_amdgcn_mov_dpp(__builtin_bit_cast(int, x), CTRL, 0xf, 0xf, true));
}
constexpr int XOR1 = 0xB1, XOR2 = 0x4E, XOR7 = 0x141, XOR8 = 0x128;
__device__ __forceinline__ f32x2 swap16(float x) {
  auto s = __builtin_amdgcn_permlane16_swap(__float_as_uint(x), __float_as_uint(x), false, false);
  return f32x2{__uint_as_float(s[0]), __uint_as_float(s[1])};
}
__device__ __forceinline__ f32x2 swap32(float x, float y) {
  auto s = __builtin_amdgcn_permlane32_swap(__float_as_uint(x), __float_as_uint(y), false, false);
  return f32x2{__uint_as_float(s[0]), __uint_as_float(s[1])};
}
__device__ __forceinline__ float wave_max(float x) {
  x = fmaxf(x, dpp<XOR1>(x)); x = fmaxf(x, dpp<XOR2>(x)); x = fmaxf(x, dpp<XOR7>(x)); x = fmaxf(x, dpp<XOR8>(x));
  f32x2 s = swap16(x);
  x = fmaxf(s[0], s[1]);
  s = swap32(x, x);
  return fmaxf(s[0], s[1]);
}
__device__ __forceinline__ void sleep_until(unsigned long long t0, unsigned ticks) {
  while ((unsigned)(__builtin_amdgcn_s_memrealtime() - t0) < ticks) __builtin_amdgcn_s_sleep(32);
}
__device__ __forceinline__ void fence() { asm volatile("" ::: "memory"); __builtin_amdgcn_sched_barrier(0); }
template <int I, int N, typename F> __device__ __forceinline__ void static_for(F&& f) {
  if constexpr (I < N) { f(std::integral_constant<int, I>{}); static_for<I + 1, N>(f); }
}

template <int J> __device__ __forceinline__ void throttle(int level) {
  constexpr int JR = ((J & 1) << 2) | (J & 2) | ((J >> 2) & 1);
  asm volatile("s_waitcnt vmcnt(6)\n\ts_cmp_gt_i32 %0, %1\n\ts_cbranch_scc1 1f\n\t"
               "s_waitcnt vmcnt(5)\n\ts_cmp_gt_i32 %0, %2\n\ts_cbranch_scc1 1f\n\t"
               "s_waitcnt vmcnt(4)\n\ts_cmp_gt_i32 %0, %3\n\ts_cbranch_scc1 1f\n\t"
               "s_waitcnt vmcnt(3)\n1:" ::"s"(level), "n"(16 + JR), "n"(8 + JR), "n"(JR) : "scc");
}
template <int GAIN> __device__ __forceinline__ int level_at(unsigned long long now, unsigned tstart, int it) {
  asm volatile("" : "+s"(now));
  const int lag = (int)((unsigned)now - tstart) - T_FIRST - ((it * T_ALL) >> 7);
  return LEVEL0 + ((lag * GAIN) >> 8);
}

__device__ __forceinline__ float reduce4(const float (&x)[4], int c) {
  const float a0 = x[0] + dpp<XOR8>(x[0]), a2 = x[2] + dpp<XOR8>(x[2]), a1 = x[1] + dpp<XOR8>(x[1]), a3 = x[3] + dpp<XOR8>(x[3]);
  const float y0 = (c & 8) ? a2 : a0, y1 = (c & 8) ? a3 : a1;
  const float b0 = y0 + dpp<XOR7>(y0), b1 = y1 + dpp<XOR7>(y1);
  float s = (c & 4) ? b1 : b0;
  s += dpp<XOR2>(s);
  s += dpp<XOR1>(s);
  const f32x2 t = swap16(s);
  return (t[0] + t[1]) * C2;
}

__global__ void __launch_bounds__((WAVES + 1) * 64, 2)
attn_decode(const u32x4* __restrict__ q, const void* k, const void* v, unsigned* ws, bf16x4* __restrict__ out) {
  __shared__ __attribute__((aligned(16))) float lds[LDS_FLOATS];
  unsigned long long t0 = __builtin_amdgcn_s_memrealtime();
  asm volatile("" ::"s"(q), "s"(k), "s"(v), "s"(ws), "s"(out));
  const int bh = blockIdx.x / NSPLIT, member = blockIdx.x % NSPLIT;
  const int lane = threadIdx.x & 63, wave = __builtin_amdgcn_readfirstlane(threadIdx.x >> 6);
  unsigned* old = reinterpret_cast<unsigned*>(lds + LDS_OLD);
  const rsrc_t rw = rsrc(ws + bh * WS_HEAD, WS_HEAD * 4);

  if (wave == WAVES) {
    __builtin_amdgcn_s_setprio(3);
    sleep_until(t0, T_NOTE);
    const unsigned a0 = __builtin_amdgcn_readfirstlane(__builtin_amdgcn_raw_buffer_load_b32(rw, WS_A * 4, 0, AUX_SC1 | AUX_NT));
    const int j0 = member == 0 ? 0 : member - 1, j1 = member == 0 ? 3 : member;
    for (int j = j0; j < j1; ++j) {
      const u32x4 o4 = __builtin_amdgcn_raw_buffer_load_b128(rw, lane * 16, j * MB * 4, AUX_SC1 | AUX_NT);
      const u32x2 ml = __builtin_amdgcn_raw_buffer_load_b64(rw, D * 4, j * MB * 4, AUX_SC1 | AUX_NT);
      *reinterpret_cast<u32x4*>(old + j * MB + lane * 4) = o4;
      *reinterpret_cast<u32x2*>(old + j * MB + D) = ml;
    }
    if (member == 0) {
      old[3 * MB] = a0 + 1u;
      asm volatile("s_waitcnt vmcnt(0) lgkmcnt(0)" ::: "memory");
      sleep_until(t0, T_CHANGE);
      if (lane == 0) __builtin_amdgcn_raw_buffer_store_b32(a0 + 1u, rw, WS_A * 4, 0, AUX_SC1);
    } else {
      sleep_until(t0, T_LOOK);
#pragma unroll 1
      for (unsigned spins = 0; spins < SPIN_CAP; ++spins) {
        asm volatile("" ::: "memory");
        if ((unsigned)__builtin_amdgcn_readfirstlane(__builtin_amdgcn_raw_buffer_load_b32(rw, WS_A * 4, 0, AUX_SC1 | AUX_NT)) != a0) break;
        __builtin_amdgcn_s_sleep(64);
      }
    }
    asm volatile("s_waitcnt vmcnt(0) lgkmcnt(0)" ::: "memory");
    return;
  }

  const unsigned voff = (lane & 31) * 16 + ((lane >> 5) << BIT_HALF);
  const unsigned base = bh * HEAD_BYTES + spread(member, BIT_WG) + spread(wave, BIT_WAVE);
  auto load = [&](rsrc_t r, unsigned s, auto jc) {
    constexpr unsigned O = spread(jc(), BIT_LOAD);
    return __builtin_amdgcn_raw_buffer_load_b128(r, voff + (O & 4095u), s + (O & ~4095u), AUX_NT);
  };
  const u32x4 qv = __builtin_nontemporal_load(q + bh * 32 + (lane & 31));
  u32x4 T[RING];
  static_for<0, RING>([&](auto j) { T[j] = load(rsrc(k, TENSOR_BYTES), base, j); });
  fence();
  asm volatile("" : "+s"(t0));
  const unsigned tstart = (unsigned)t0;
  const int c = lane & 15;
  float* sc = lds + wave * NIT * RING * 2;
  int level = 16;

  float m = NEG_BIG;
  {
    float* w = sc + (lane >> 5) * 4 + (c >> 2);
#pragma unroll 1
    for (int it = 0; it < NIT; ++it, w += RING * 2) {
      const unsigned long long now = __builtin_amdgcn_s_memrealtime();
      const bool last = it == NIT - 1;
      const rsrc_t r = rsrc(last ? v : k, TENSOR_BYTES);
      const unsigned s = base + spread(it + 1, BIT_IT);
      level = last ? LEVEL_MAX : level;
      float x[4];
      fence();
      static_for<0, RING>([&](auto jc) {
        constexpr int J = jc;
        float d = 0.f;
#pragma unroll
        for (int i = 0; i < 4; ++i) d = dot2(T[J][i], qv[i], d);
        x[J % 4] = d;
        asm volatile("" : "+v"(x[J % 4]));
        throttle<J>(level);
        T[J] = load(r, s, jc);
        fence();
        if constexpr (J % 4 == 3) {
          const float sv = reduce4(x, c);
          m = fmaxf(m, sv);
          w[(J / 4) * 8] = sv;
          fence();
        }
      });
      level = level_at<GAIN_K>(now, tstart, it);
    }
  }
  m = wave_max(m);
  fence();

  float l = 0.f;
  f32x2 acc[4] = {{0.f, 0.f}, {0.f, 0.f}, {0.f, 0.f}, {0.f, 0.f}};
  {
    const f32x4* pr = reinterpret_cast<const f32x4*>(sc) + (lane >> 5);
    f32x4 s0 = pr[0], s1;
#pragma unroll 1
    for (int it = 0; it < NIT; ++it) {
      unsigned long long now;
      const bool last = it == NIT - 1;
      const rsrc_t r = rsrc(v, last ? 0u : TENSOR_BYTES);
      const unsigned s = base + spread(it + 1, BIT_IT);
      level = last ? LEVEL_MAX : level;
      fence();
      static_for<0, RING>([&](auto jc) {
        constexpr int J = jc;
        if constexpr (J == 1) { now = __builtin_amdgcn_s_memrealtime(); s1 = pr[2]; }
        if constexpr (J == 4) { pr += 4; s0 = pr[0]; }
        const float p = __builtin_amdgcn_exp2f((J < 4 ? s0[J % 4] : s1[J % 4]) - m);
        l += p;
        const f32x2 pp = {p, p};
#pragma unroll
        for (int i = 0; i < 4; ++i) {
          const unsigned w = T[J][i];
          acc[i] = __builtin_elementwise_fma(pp, f32x2{__uint_as_float(w << 16), __uint_as_float(w & 0xffff0000u)}, acc[i]);
        }
        asm volatile("" : "+v"(acc[0]), "+v"(acc[1]), "+v"(acc[2]), "+v"(acc[3]));
        throttle<J>(level);
        T[J] = load(r, s, jc);
        fence();
      });
      level = level_at<GAIN_V>(now, tstart, NIT + it);
    }
  }

  float* mrg = lds + LDS_MRG;
  {
    f32x4 h;
#pragma unroll
    for (int i = 0; i < 4; ++i) {
      const f32x2 t = swap32(acc[i / 2][i % 2], acc[2 + i / 2][i % 2]);
      h[i] = t[0] + t[1];
    }
    const f32x2 l2 = swap32(l, l);
    float* rec = mrg + wave * REC;
    *reinterpret_cast<f32x4*>(rec + 4 + 8 * (lane & 31) + 4 * (lane >> 5)) = h;
    *reinterpret_cast<f32x2*>(rec) = f32x2{m, l2[0] + l2[1]};
  }
  asm volatile("s_waitcnt lgkmcnt(0)" ::: "memory");
  __builtin_amdgcn_s_barrier();
  asm volatile("" ::: "memory");
  if (wave != 0) return;
  f32x2 ml[WAVES];
  f32x4 ow[WAVES];
  u32x4 so[3];
  u32x2 sm[3];
#pragma unroll
  for (int w = 0; w < WAVES; ++w) {
    ml[w] = *reinterpret_cast<const f32x2*>(mrg + w * REC);
    ow[w] = *reinterpret_cast<const f32x4*>(mrg + w * REC + 4 + 4 * lane);
  }
#pragma unroll
  for (int j = 0; j < 3; ++j) {
    const unsigned* od = old + (j == 0 && member != 0 ? member - 1 : j) * MB;
    so[j] = *reinterpret_cast<const u32x4*>(od + 4 * lane);
    sm[j] = *reinterpret_cast<const u32x2*>(od + D);
  }
  unsigned a = old[3 * MB];
  fence();
  float M = fmaxf(fmaxf(ml[0][0], ml[1][0]), fmaxf(ml[2][0], ml[3][0]));
  M = __uint_as_float(__float_as_uint(M) & ~1u);
  float L = 0.f;
  f32x4 o = {0.f, 0.f, 0.f, 0.f};
#pragma unroll
  for (int w = 0; w < WAVES; ++w) {
    const float wt = __builtin_amdgcn_exp2f(ml[w][0] - M);
    L = fmaf(wt, ml[w][1], L);
    o += wt * ow[w];
  }
  if (member != 0) {
    const u32x4 wo = (__builtin_bit_cast(u32x4, o) & ~1u) | (~so[0] & 1u);
    const u32x2 wm = (u32x2{__float_as_uint(M), __float_as_uint(L)} & ~1u) | (~sm[0] & 1u);
    __builtin_amdgcn_raw_buffer_store_b128(wo, rw, lane * 16, (member - 1) * MB * 4, AUX_SC1);
    __builtin_amdgcn_raw_buffer_store_b64(wm, rw, D * 4 + lane * (WS_HEAD * 4), (member - 1) * MB * 4, AUX_SC1);
    return;
  }

  u32x4 uo[3];
  u32x2 um[3];
#pragma unroll 1
  for (unsigned spins = 1; spins < SPIN_CAP; ++spins) {
#pragma unroll
    for (int j = 0; j < 3; ++j) {
      uo[j] = __builtin_amdgcn_raw_buffer_load_b128(rw, lane * 16, j * MB * 4, AUX_SC1 | AUX_NT);
      um[j] = __builtin_amdgcn_raw_buffer_load_b64(rw, D * 4, j * MB * 4, AUX_SC1 | AUX_NT);
    }
    fence();
    unsigned ok = 1u;
#pragma unroll
    for (int j = 0; j < 3; ++j) {
      const u32x4 x4 = uo[j] ^ so[j];
      const u32x2 x2 = um[j] ^ sm[j];
      ok &= x4[0] & x4[1] & x4[2] & x4[3] & x2[0] & x2[1];
    }
    if (__all(ok & 1u)) break;
    if ((spins & 63u) == 0u && lane == 0)
      __builtin_amdgcn_raw_buffer_store_b32(++a, rw, WS_A * 4, 0, AUX_SC1);
  }
  float MM = M;
#pragma unroll
  for (int j = 0; j < 3; ++j) MM = fmaxf(MM, __uint_as_float(um[j][0] & ~1u));
  const float w0 = __builtin_amdgcn_exp2f(M - MM);
  L *= w0;
  o *= w0;
#pragma unroll
  for (int j = 0; j < 3; ++j) {
    const float wt = __builtin_amdgcn_exp2f(__uint_as_float(um[j][0] & ~1u) - MM);
    L = fmaf(wt, __uint_as_float(um[j][1] & ~1u), L);
    o += wt * __builtin_bit_cast(f32x4, uo[j] & ~1u);
  }
  out[bh * 64 + lane] = __builtin_convertvector(o * (1.f / L), bf16x4);
}

extern "C" void kernel_launch(void* const* d_in, const int* in_sizes, int n_in, void* d_out, int out_size, void* d_ws,
                              size_t ws_size, hipStream_t stream) {
  attn_decode<<<BH * NSPLIT, (WAVES + 1) * 64, 0, stream>>>((const u32x4*)d_in[0], d_in[1], d_in[2], (unsigned*)d_ws, (bf16x4*)d_out);
}
```

```cpp
#include <hip/hip_runtime.h>
#include <hip/hip_bf16.h>
#include <stdint.h>
#include <type_traits>

constexpr int BH = 64, SKV = 16384, D = 256;
constexpr int NSPLIT = 4, WAVES = 4;
constexpr unsigned HEAD_BYTES = SKV * D * 2, TENSOR_BYTES = BH * HEAD_BYTES;
constexpr int RING = 8;
constexpr int NIT = SKV / (NSPLIT * WAVES * RING * 2);
constexpr unsigned MASK[20] = {0x0090, 0x2ff3, 0x22e4, 0x0011, 0x1000, 0x0051, 0x3473, 0x3560, 0x0f3d, 0x0666, 0x0bcf, 0x32b1, 0x16c4, 0x24ad, 0x34f0, 0x3087, 0x1127, 0x0c46, 0x2949, 0x37a0};
constexpr int M_HALF = 0, M_WAVE = 1, M_LOAD = 3, M_IT = 6, M_WG = 12, M_HEAD = 14;
template <int FIRST, int N> __host__ __device__ constexpr unsigned key_bytes(unsigned x) {
  unsigned r = 0;
  for (int b = 0; b < N; ++b) r ^= (0u - ((x >> b) & 1u)) & (MASK[FIRST + b] * (D * 2));
  return r;
}
constexpr bool masks_invertible() {
  unsigned m[14] = {};
  for (int i = 0; i < 14; ++i) m[i] = MASK[i];
  for (int c = 0; c < 14; ++c) {
    int p = c;
    while (p < 14 && !((m[p] >> c) & 1u)) ++p;
    if (p == 14) return false;
    const unsigned t = m[p]; m[p] = m[c]; m[c] = t;
    for (int i = c + 1; i < 14; ++i) if ((m[i] >> c) & 1u) m[i] ^= t;
  }
  return true;
}
static_assert(masks_invertible() && SKV == 1 << 14, "every key exactly once");
constexpr float C2 = 0.0625f * 1.4426950408889634f;
constexpr float NEG_BIG = -3.4028234663852886e38f;
constexpr int T_ALL = 13600, T_FIRST = 100, LEVEL0 = 11, LEVEL_MAX = 24, GAIN_K = 8, GAIN_V = 12;
constexpr unsigned T_NOTE = 7200, T_CHANGE = 8000, T_LOOK = 8400;
constexpr int MB = 288, WS_A = 3 * MB, WS_HEAD = WS_A + 32;
constexpr unsigned SPIN_CAP = 1u << 18;
constexpr int AUX_NT = 2, AUX_SC1 = 16;
constexpr int REC = D + 4, LDS_MRG = WAVES * NIT * RING * 2, LDS_OLD = LDS_MRG + WAVES * REC, LDS_FLOATS = LDS_OLD + 3 * MB + 4;

using u32x2 = __attribute__((ext_vector_type(2))) unsigned;
using u32x4 = __attribute__((ext_vector_type(4))) unsigned;
using f32x2 = __attribute__((ext_vector_type(2))) float;
using f32x4 = __attribute__((ext_vector_type(4))) float;
using bf16x2 = __attribute__((ext_vector_type(2))) __bf16;
using bf16x4 = __attribute__((ext_vector_type(4))) __bf16;
using rsrc_t = __amdgpu_buffer_rsrc_t;

__device__ __forceinline__ rsrc_t rsrc(const void* p, unsigned bytes) {
  return __builtin_amdgcn_make_buffer_rsrc((void*)p, 0, (int)bytes, 0x00020000);
}
__device__ __forceinline__ float dot2(unsigned k, unsigned q, float acc) {
  return __builtin_amdgcn_fdot2_f32_bf16(__builtin_bit_cast(bf16x2, k), __builtin_bit_cast(bf16x2, q), acc, false);
}
template <int CTRL> __device__ __forceinline__ float dpp(float x) {
  return __builtin_bit_cast(float, __builtin_amdgcn_mov_dpp(__builtin_bit_cast(int, x), CTRL, 0xf, 0xf, true));
}
constexpr int XOR1 = 0xB1, XOR2 = 0x4E, XOR7 = 0x141, XOR8 = 0x128;
__device__ __forceinline__ f32x2 swap16(float x) {
  auto s = __builtin_amdgcn_permlane16_swap(__float_as_uint(x), __float_as_uint(x), false, false);
  return f32x2{__uint_as_float(s[0]), __uint_as_float(s[1])};
}
__device__ __forceinline__ f32x2 swap32(float x, float y) {
  auto s = __builtin_amdgcn_permlane32_swap(__float_as_uint(x), __float_as_uint(y), false, false);
  return f32x2{__uint_as_float(s[0]), __uint_as_float(s[1])};
}
__device__ __forceinline__ float wave_max(float x) {
  x = fmaxf(x, dpp<XOR1>(x)); x = fmaxf(x, dpp<XOR2>(x)); x = fmaxf(x, dpp<XOR7>(x)); x = fmaxf(x, dpp<XOR8>(x));
  f32x2 s = swap16(x);
  x = fmaxf(s[0], s[1]);
  s = swap32(x, x);
  return fmaxf(s[0], s[1]);
}
__device__ __forceinline__ void sleep_until(unsigned long long t0, unsigned ticks) {
  while ((unsigned)(__builtin_amdgcn_s_memrealtime() - t0) < ticks) __builtin_amdgcn_s_sleep(32);
}
__device__ __forceinline__ void fence() { asm volatile("" ::: "memory"); __builtin_amdgcn_sched_barrier(0); }
template <int I, int N, typename F> __device__ __forceinline__ void static_for(F&& f) {
  if constexpr (I < N) { f(std::integral_constant<int, I>{}); static_for<I + 1, N>(f); }
}

template <int J> __device__ __forceinline__ void throttle(int level) {
  constexpr int JR = ((J & 1) << 2) | (J & 2) | ((J >> 2) & 1);
  asm volatile("s_waitcnt vmcnt(6)\n\ts_cmp_gt_i32 %0, %1\n\ts_cbranch_scc1 1f\n\t"
               "s_waitcnt vmcnt(5)\n\ts_cmp_gt_i32 %0, %2\n\ts_cbranch_scc1 1f\n\t"
               "s_waitcnt vmcnt(4)\n\ts_cmp_gt_i32 %0, %3\n\ts_cbranch_scc1 1f\n\t"
               "s_waitcnt vmcnt(3)\n1:" ::"s"(level), "n"(16 + JR), "n"(8 + JR), "n"(JR) : "scc");
}
template <int GAIN> __device__ __forceinline__ int level_at(unsigned long long now, unsigned tstart, int it) {
  asm volatile("" : "+s"(now));
  const int lag = (int)((unsigned)now - tstart) - T_FIRST - ((it * T_ALL) >> 7);
  return LEVEL0 + ((lag * GAIN) >> 8);
}

__device__ __forceinline__ float reduce4(const float (&x)[4], int c) {
  const float a0 = x[0] + dpp<XOR8>(x[0]), a2 = x[2] + dpp<XOR8>(x[2]), a1 = x[1] + dpp<XOR8>(x[1]), a3 = x[3] + dpp<XOR8>(x[3]);
  const float y0 = (c & 8) ? a2 : a0, y1 = (c & 8) ? a3 : a1;
  const float b0 = y0 + dpp<XOR7>(y0), b1 = y1 + dpp<XOR7>(y1);
  float s = (c & 4) ? b1 : b0;
  s += dpp<XOR2>(s);
  s += dpp<XOR1>(s);
  const f32x2 t = swap16(s);
  return (t[0] + t[1]) * C2;
}

__global__ void __launch_bounds__((WAVES + 1) * 64, 2)
attn_decode(const u32x4* __restrict__ q, const void* k, const void* v, unsigned* ws, bf16x4* __restrict__ out) {
  __shared__ __attribute__((aligned(16))) float lds[LDS_FLOATS];
  unsigned long long t0 = __builtin_amdgcn_s_memrealtime();
  asm volatile("" ::"s"(q), "s"(k), "s"(v), "s"(ws), "s"(out));
  const int bh = blockIdx.x / NSPLIT, member = blockIdx.x % NSPLIT;
  const int lane = threadIdx.x & 63, wave = __builtin_amdgcn_readfirstlane(threadIdx.x >> 6);
  unsigned* old = reinterpret_cast<unsigned*>(lds + LDS_OLD);
  const rsrc_t rw = rsrc(ws + bh * WS_HEAD, WS_HEAD * 4);

  if (wave == WAVES) {
    __builtin_amdgcn_s_setprio(3);
    sleep_until(t0, T_NOTE);
    const unsigned a0 = __builtin_amdgcn_readfirstlane(__builtin_amdgcn_raw_buffer_load_b32(rw, WS_A * 4, 0, AUX_SC1 | AUX_NT));
    const int j0 = member == 0 ? 0 : member - 1, j1 = member == 0 ? 3 : member;
    for (int j = j0; j < j1; ++j) {
      const u32x4 o4 = __builtin_amdgcn_raw_buffer_load_b128(rw, lane * 16, j * MB * 4, AUX_SC1 | AUX_NT);
      const u32x2 ml = __builtin_amdgcn_raw_buffer_load_b64(rw, D * 4, j * MB * 4, AUX_SC1 | AUX_NT);
      *reinterpret_cast<u32x4*>(old + j * MB + lane * 4) = o4;
      *reinterpret_cast<u32x2*>(old + j * MB + D) = ml;
    }
    if (member == 0) {
      old[3 * MB] = a0 + 1u;
      asm volatile("s_waitcnt vmcnt(0) lgkmcnt(0)" ::: "memory");
      sleep_until(t0, T_CHANGE);
      if (lane == 0) __builtin_amdgcn_raw_buffer_store_b32(a0 + 1u, rw, WS_A * 4, 0, AUX_SC1);
    } else {
      sleep_until(t0, T_LOOK);
#pragma unroll 1
      for (unsigned spins = 0; spins < SPIN_CAP; ++spins) {
        asm volatile("" ::: "memory");
        if ((unsigned)__builtin_amdgcn_readfirstlane(__builtin_amdgcn_raw_buffer_load_b32(rw, WS_A * 4, 0, AUX_SC1 | AUX_NT)) != a0) break;
        __builtin_amdgcn_s_sleep(64);
      }
    }
    asm volatile("s_waitcnt vmcnt(0) lgkmcnt(0)" ::: "memory");
    return;
  }

  const unsigned voff = ((lane & 31) * 16) | (key_bytes<M_HALF, 1>(lane >> 5) ^ key_bytes<M_WAVE, 2>(wave) ^ key_bytes<M_WG, 2>(member) ^ key_bytes<M_HEAD, 6>(bh));
  auto load = [&](rsrc_t r, unsigned s, auto j) {
    return __builtin_amdgcn_raw_buffer_load_b128(r, voff ^ s ^ key_bytes<M_LOAD, 3>(j), bh * HEAD_BYTES, AUX_NT);
  };
  const u32x4 qv = __builtin_nontemporal_load(q + bh * 32 + (lane & 31));
  u32x4 T[RING];
  static_for<0, RING>([&](auto j) { T[j] = load(rsrc(k, TENSOR_BYTES), 0u, j); });
  fence();
  asm volatile("" : "+s"(t0));
  const unsigned tstart = (unsigned)t0;
  const int c = lane & 15;
  float* sc = lds + wave * NIT * RING * 2;
  int level = 16;

  float m = NEG_BIG;
  {
    float* w = sc + (lane >> 5) * 4 + (c >> 2);
#pragma unroll 1
    for (int it = 0; it < NIT; ++it, w += RING * 2) {
      const unsigned long long now = __builtin_amdgcn_s_memrealtime();
      const bool last = it == NIT - 1;
      const rsrc_t r = rsrc(last ? v : k, TENSOR_BYTES);
      const unsigned s = key_bytes<M_IT, 6>(it + 1);
      level = last ? LEVEL_MAX : level;
      float x[4];
      fence();
      static_for<0, RING>([&](auto jc) {
        constexpr int J = jc;
        float d = 0.f;
#pragma unroll
        for (int i = 0; i < 4; ++i) d = dot2(T[J][i], qv[i], d);
        x[J % 4] = d;
        asm volatile("" : "+v"(x[J % 4]));
        throttle<J>(level);
        T[J] = load(r, s, jc);
        fence();
        if constexpr (J % 4 == 3) {
          const float sv = reduce4(x, c);
          m = fmaxf(m, sv);
          w[(J / 4) * 8] = sv;
          fence();
        }
      });
      level = level_at<GAIN_K>(now, tstart, it);
    }
  }
  m = wave_max(m);
  fence();

  float l = 0.f;
  f32x2 acc[4] = {{0.f, 0.f}, {0.f, 0.f}, {0.f, 0.f}, {0.f, 0.f}};
  {
    const f32x4* pr = reinterpret_cast<const f32x4*>(sc) + (lane >> 5);
    f32x4 s0 = pr[0], s1;
#pragma unroll 1
    for (int it = 0; it < NIT; ++it) {
      unsigned long long now;
      const bool last = it == NIT - 1;
      const rsrc_t r = rsrc(v, last ? 0u : TENSOR_BYTES);
      const unsigned s = key_bytes<M_IT, 6>(it + 1);
      level = last ? LEVEL_MAX : level;
      fence();
      static_for<0, RING>([&](auto jc) {
        constexpr int J = jc;
        if constexpr (J == 1) { now = __builtin_amdgcn_s_memrealtime(); s1 = pr[2]; }
        if constexpr (J == 4) { pr += 4; s0 = pr[0]; }
        const float p = __builtin_amdgcn_exp2f((J < 4 ? s0[J % 4] : s1[J % 4]) - m);
        l += p;
        const f32x2 pp = {p, p};
#pragma unroll
        for (int i = 0; i < 4; ++i) {
          const unsigned w = T[J][i];
          acc[i] = __builtin_elementwise_fma(pp, f32x2{__uint_as_float(w << 16), __uint_as_float(w & 0xffff0000u)}, acc[i]);
        }
        asm volatile("" : "+v"(acc[0]), "+v"(acc[1]), "+v"(acc[2]), "+v"(acc[3]));
        throttle<J>(level);
        T[J] = load(r, s, jc);
        fence();
      });
      level = level_at<GAIN_V>(now, tstart, NIT + it);
    }
  }

  float* mrg = lds + LDS_MRG;
  {
    f32x4 h;
#pragma unroll
    for (int i = 0; i < 4; ++i) {
      const f32x2 t = swap32(acc[i / 2][i % 2], acc[2 + i / 2][i % 2]);
      h[i] = t[0] + t[1];
    }
    const f32x2 l2 = swap32(l, l);
    float* rec = mrg + wave * REC;
    *reinterpret_cast<f32x4*>(rec + 4 + 8 * (lane & 31) + 4 * (lane >> 5)) = h;
    *reinterpret_cast<f32x2*>(rec) = f32x2{m, l2[0] + l2[1]};
  }
  asm volatile("s_waitcnt lgkmcnt(0)" ::: "memory");
  __builtin_amdgcn_s_barrier();
  asm volatile("" ::: "memory");
  if (wave != 0) return;
  f32x2 ml[WAVES];
  f32x4 ow[WAVES];
  u32x4 so[3];
  u32x2 sm[3];
#pragma unroll
  for (int w = 0; w < WAVES; ++w) {
    ml[w] = *reinterpret_cast<const f32x2*>(mrg + w * REC);
    ow[w] = *reinterpret_cast<const f32x4*>(mrg + w * REC + 4 + 4 * lane);
  }
#pragma unroll
  for (int j = 0; j < 3; ++j) {
    const unsigned* od = old + (j == 0 && member != 0 ? member - 1 : j) * MB;
    so[j] = *reinterpret_cast<const u32x4*>(od + 4 * lane);
    sm[j] = *reinterpret_cast<const u32x2*>(od + D);
  }
  unsigned a = old[3 * MB];
  fence();
  float M = fmaxf(fmaxf(ml[0][0], ml[1][0]), fmaxf(ml[2][0], ml[3][0]));
  M = __uint_as_float(__float_as_uint(M) & ~1u);
  float L = 0.f;
  f32x4 o = {0.f, 0.f, 0.f, 0.f};
#pragma unroll
  for (int w = 0; w < WAVES; ++w) {
    const float wt = __builtin_amdgcn_exp2f(ml[w][0] - M);
    L = fmaf(wt, ml[w][1], L);
    o += wt * ow[w];
  }
  if (member != 0) {
    const u32x4 wo = (__builtin_bit_cast(u32x4, o) & ~1u) | (~so[0] & 1u);
    const u32x2 wm = (u32x2{__float_as_uint(M), __float_as_uint(L)} & ~1u) | (~sm[0] & 1u);
    __builtin_amdgcn_raw_buffer_store_b128(wo, rw, lane * 16, (member - 1) * MB * 4, AUX_SC1);
    __builtin_amdgcn_raw_buffer_store_b64(wm, rw, D * 4 + lane * (WS_HEAD * 4), (member - 1) * MB * 4, AUX_SC1);
    return;
  }

  u32x4 uo[3];
  u32x2 um[3];
#pragma unroll 1
  for (unsigned spins = 1; spins < SPIN_CAP; ++spins) {
#pragma unroll
    for (int j = 0; j < 3; ++j) {
      uo[j] = __builtin_amdgcn_raw_buffer_load_b128(rw, lane * 16, j * MB * 4, AUX_SC1 | AUX_NT);
      um[j] = __builtin_amdgcn_raw_buffer_load_b64(rw, D * 4, j * MB * 4, AUX_SC1 | AUX_NT);
    }
    fence();
    unsigned ok = 1u;
#pragma unroll
    for (int j = 0; j < 3; ++j) {
      const u32x4 x4 = uo[j] ^ so[j];
      const u32x2 x2 = um[j] ^ sm[j];
      ok &= x4[0] & x4[1] & x4[2] & x4[3] & x2[0] & x2[1];
    }
    if (__all(ok & 1u)) break;
    if ((spins & 63u) == 0u && lane == 0)
      __builtin_amdgcn_raw_buffer_store_b32(++a, rw, WS_A * 4, 0, AUX_SC1);
  }
  float MM = M;
#pragma unroll
  for (int j = 0; j < 3; ++j) MM = fmaxf(MM, __uint_as_float(um[j][0] & ~1u));
  const float w0 = __builtin_amdgcn_exp2f(M - MM);
  L *= w0;
  o *= w0;
#pragma unroll
  for (int j = 0; j < 3; ++j) {
    const float wt = __builtin_amdgcn_exp2f(__uint_as_float(um[j][0] & ~1u) - MM);
    L = fmaf(wt, __uint_as_float(um[j][1] & ~1u), L);
    o += wt * __builtin_bit_cast(f32x4, uo[j] & ~1u);
  }
  out[bh * 64 + lane] = __builtin_convertvector(o * (1.f / L), bf16x4);
}

extern "C" void kernel_launch(void* const* d_in, const int* in_sizes, int n_in, void* d_out, int out_size, void* d_ws,
                              size_t ws_size, hipStream_t stream) {
  attn_decode<<<BH * NSPLIT, (WAVES + 1) * 64, 0, stream>>>((const u32x4*)d_in[0], d_in[1], d_in[2], (unsigned*)d_ws, (bf16x4*)d_out);
}
```

```cpp
#include <hip/hip_runtime.h>
#include <hip/hip_bf16.h>
#include <stdint.h>
#include <type_traits>

constexpr int BH = 64, SKV = 16384, D = 256;
constexpr int NSPLIT = 4, WAVES = 4;
constexpr unsigned HEAD_BYTES = SKV * D * 2, TENSOR_BYTES = BH * HEAD_BYTES;
constexpr int RING = 8;
constexpr int NIT = SKV / (NSPLIT * WAVES * RING * 2);
constexpr unsigned MASK[20] = {0x0090, 0x2ff3, 0x22e4, 0x0011, 0x1000, 0x0051, 0x3473, 0x3560, 0x0f3d, 0x0666,
                               0x0bcf, 0x32b1, 0x16c4, 0x24ad, 0x34f0, 0x3087, 0x1127, 0x0c46, 0x2949, 0x37a0};
constexpr int M_HALF = 0, M_WAVE = 1, M_LOAD = 3, M_IT = 6, M_WG = 12, M_HEAD = 14;
template <int FIRST, int N> __host__ __device__ constexpr unsigned key_bytes(unsigned x) {
  unsigned r = 0;
  for (int b = 0; b < N; ++b) r ^= (0u - ((x >> b) & 1u)) & (MASK[FIRST + b] * (D * 2));
  return r;
}
constexpr bool masks_invertible() {
  unsigned m[14] = {};
  for (int i = 0; i < 14; ++i) m[i] = MASK[i];
  for (int c = 0; c < 14; ++c) {
    int p = c;
    while (p < 14 && !((m[p] >> c) & 1u)) ++p;
    if (p == 14) return false;
    const unsigned t = m[p]; m[p] = m[c]; m[c] = t;
    for (int i = c + 1; i < 14; ++i) if ((m[i] >> c) & 1u) m[i] ^= t;
  }
  return true;
}
static_assert(masks_invertible() && SKV == 1 << 14, "every key exactly once");
constexpr float C2 = 0.0625f * 1.4426950408889634f;
constexpr float NEG_BIG = -3.4028234663852886e38f;
constexpr int T_ALL = 13600, T_FIRST = 100, LEVEL0 = 11, LEVEL_MAX = 24, T_PUSH = 35, GAIN_K = 8, GAIN_V = 12;
constexpr int NCK = 4, NCK1 = 2;
constexpr unsigned T_HINT0 = 400, T_HINT1 = 2200, T_HINT_STEP = 3200, T_HINT_READ = 250;
constexpr unsigned T_NOTE = 7200, T_CHANGE = 8000, T_LOOK = 8400;
constexpr int MB = 288, WS_A = 3 * MB, WS_HEAD = WS_A + 32, WS_HINT = BH * WS_HEAD;
constexpr unsigned SPIN_CAP = 1u << 18;
constexpr int AUX_NT = 2, AUX_SC1 = 16;
constexpr int REC = D + 4, LDS_MRG = WAVES * NIT * RING * 2, LDS_OLD = LDS_MRG + WAVES * REC, LDS_FLOATS = LDS_OLD + 3 * MB + 8;

using u32x2 = __attribute__((ext_vector_type(2))) unsigned;
using u32x4 = __attribute__((ext_vector_type(4))) unsigned;
using f32x2 = __attribute__((ext_vector_type(2))) float;
using f32x4 = __attribute__((ext_vector_type(4))) float;
using bf16x2 = __attribute__((ext_vector_type(2))) __bf16;
using bf16x4 = __attribute__((ext_vector_type(4))) __bf16;
using rsrc_t = __amdgpu_buffer_rsrc_t;

__device__ __forceinline__ rsrc_t rsrc(const void* p, unsigned bytes) {
  return __builtin_amdgcn_make_buffer_rsrc((void*)p, 0, (int)bytes, 0x00020000);
}
__device__ __forceinline__ float dot2(unsigned k, unsigned q, float acc) {
  return __builtin_amdgcn_fdot2_f32_bf16(__builtin_bit_cast(bf16x2, k), __builtin_bit_cast(bf16x2, q), acc, false);
}
template <int CTRL> __device__ __forceinline__ float dpp(float x) {
  return __builtin_bit_cast(float, __builtin_amdgcn_mov_dpp(__builtin_bit_cast(int, x), CTRL, 0xf, 0xf, true));
}
constexpr int XOR1 = 0xB1, XOR2 = 0x4E, XOR7 = 0x141, XOR8 = 0x128;
__device__ __forceinline__ f32x2 swap16(float x) {
  auto s = __builtin_amdgcn_permlane16_swap(__float_as_uint(x), __float_as_uint(x), false, false);
  return f32x2{__uint_as_float(s[0]), __uint_as_float(s[1])};
}
__device__ __forceinline__ f32x2 swap32(float x, float y) {
  auto s = __builtin_amdgcn_permlane32_swap(__float_as_uint(x), __float_as_uint(y), false, false);
  return f32x2{__uint_as_float(s[0]), __uint_as_float(s[1])};
}
__device__ __forceinline__ float wave_max(float x) {
  x = fmaxf(x, dpp<XOR1>(x)); x = fmaxf(x, dpp<XOR2>(x)); x = fmaxf(x, dpp<XOR7>(x)); x = fmaxf(x, dpp<XOR8>(x));
  f32x2 s = swap16(x);
  x = fmaxf(s[0], s[1]);
  s = swap32(x, x);
  return fmaxf(s[0], s[1]);
}
__device__ __forceinline__ void sleep_until(unsigned long long t0, unsigned ticks) {
  while ((unsigned)(__builtin_amdgcn_s_memrealtime() - t0) < ticks) __builtin_amdgcn_s_sleep(8);
}
__device__ __forceinline__ void fence() { asm volatile("" ::: "memory"); __builtin_amdgcn_sched_barrier(0); }
template <int I, int N, typename F> __device__ __forceinline__ void static_for(F&& f) {
  if constexpr (I < N) { f(std::integral_constant<int, I>{}); static_for<I + 1, N>(f); }
}

template <int J> __device__ __forceinline__ void throttle(int level) {
  constexpr int JR = ((J & 1) << 2) | (J & 2) | ((J >> 2) & 1);
  asm volatile("s_waitcnt vmcnt(6)\n\ts_cmp_gt_i32 %0, %1\n\ts_cbranch_scc1 1f\n\t"
               "s_waitcnt vmcnt(5)\n\ts_cmp_gt_i32 %0, %2\n\ts_cbranch_scc1 1f\n\t"
               "s_waitcnt vmcnt(4)\n\ts_cmp_gt_i32 %0, %3\n\ts_cbranch_scc1 1f\n\t"
               "s_waitcnt vmcnt(3)\n1:" ::"s"(level), "n"(16 + JR), "n"(8 + JR), "n"(JR) : "scc");
}
template <int GAIN> __device__ __forceinline__ int level_at(unsigned long long now, int it, unsigned ref0, unsigned ref, unsigned* progress) {
  asm volatile("" : "+s"(now));
  const unsigned p = (unsigned)now - ((it * T_ALL) >> 7);
  *progress = p;
  const unsigned r = __builtin_amdgcn_readfirstlane(ref);
  const int lag = (int)(p - (it >= 8 && r - ref0 + 2048u < 4096u ? r : ref0));
  return LEVEL0 + ((lag * GAIN) >> 8);
}

__device__ __forceinline__ float reduce4(const float (&x)[4], int c) {
  const float a0 = x[0] + dpp<XOR8>(x[0]), a2 = x[2] + dpp<XOR8>(x[2]), a1 = x[1] + dpp<XOR8>(x[1]), a3 = x[3] + dpp<XOR8>(x[3]);
  const float y0 = (c & 8) ? a2 : a0, y1 = (c & 8) ? a3 : a1;
  const float b0 = y0 + dpp<XOR7>(y0), b1 = y1 + dpp<XOR7>(y1);
  float s = (c & 4) ? b1 : b0;
  s += dpp<XOR2>(s);
  s += dpp<XOR1>(s);
  const f32x2 t = swap16(s);
  return (t[0] + t[1]) * C2;
}

__global__ void __launch_bounds__((WAVES + 1) * 64, 2)
attn_decode(const u32x4* __restrict__ q, const void* k, const void* v, unsigned* ws, bf16x4* __restrict__ out) {
  __shared__ __attribute__((aligned(16))) float lds[LDS_FLOATS];
  unsigned long long t0 = __builtin_amdgcn_s_memrealtime();
  asm volatile("" ::"s"(q), "s"(k), "s"(v), "s"(ws), "s"(out));
  const int bh = blockIdx.x / NSPLIT, member = blockIdx.x % NSPLIT;
  const int lane = threadIdx.x & 63, wave = __builtin_amdgcn_readfirstlane(threadIdx.x >> 6);
  unsigned* old = reinterpret_cast<unsigned*>(lds + LDS_OLD);
  const rsrc_t rw = rsrc(ws + bh * WS_HEAD, WS_HEAD * 4);

  if (wave == WAVES) {
    __builtin_amdgcn_s_setprio(3);
    old[3 * MB + 1] = (unsigned)t0 + T_FIRST;
    const rsrc_t rh = rsrc(ws + WS_HINT, NCK * 8 * 4);
    sleep_until(t0, T_HINT0 + blockIdx.x);
    const unsigned h0 = __builtin_amdgcn_raw_buffer_load_b32(rh, lane * 4, 0, AUX_SC1);
    auto recentre = [&](int c) {
      sleep_until(t0, T_HINT1 + c * T_HINT_STEP);
      if (blockIdx.x < 8) {
        asm volatile("" ::: "memory");
        const u32x4 p = *reinterpret_cast<const u32x4*>(old + 3 * MB + 4);
        const unsigned mean = p[0] + (unsigned)((int)(p[1] + p[2] + p[3] - 3u * p[0]) >> 2);
        if (lane == c * 8 + blockIdx.x) __builtin_amdgcn_raw_buffer_store_b32((mean & ~1u) | (~h0 & 1u), rh, lane * 4, 0, AUX_SC1);
      }
      sleep_until(t0, T_HINT1 + c * T_HINT_STEP + T_HINT_READ + blockIdx.x);
      asm volatile("" ::: "memory");
      const unsigned h = __builtin_amdgcn_raw_buffer_load_b32(rh, lane * 4, 0, AUX_SC1);
      if (((__ballot((h ^ h0) & 1u) >> (c * 8)) & 0xffu) == 0xffu) {
        unsigned sum = 0;
#pragma unroll
        for (int b = 0; b < 8; ++b) sum += __builtin_amdgcn_readlane(h, c * 8 + b) - (unsigned)t0;
        old[3 * MB + 1] = (unsigned)t0 + (unsigned)max(((int)sum >> 3) - T_PUSH, T_FIRST);
      }
    };
#pragma unroll 1
    for (int c = 0; c < NCK1; ++c) recentre(c);
    sleep_until(t0, T_NOTE);
    const unsigned a0 = __builtin_amdgcn_readfirstlane(__builtin_amdgcn_raw_buffer_load_b32(rw, WS_A * 4, 0, AUX_SC1 | AUX_NT));
    const int j0 = member == 0 ? 0 : member - 1, j1 = member == 0 ? 3 : member;
    for (int j = j0; j < j1; ++j) {
      const u32x4 o4 = __builtin_amdgcn_raw_buffer_load_b128(rw, lane * 16, j * MB * 4, AUX_SC1 | AUX_NT);
      const u32x2 ml = __builtin_amdgcn_raw_buffer_load_b64(rw, D * 4, j * MB * 4, AUX_SC1 | AUX_NT);
      *reinterpret_cast<u32x4*>(old + j * MB + lane * 4) = o4;
      *reinterpret_cast<u32x2*>(old + j * MB + D) = ml;
    }
    if (member == 0) {
      old[3 * MB] = a0 + 1u;
      asm volatile("s_waitcnt vmcnt(0) lgkmcnt(0)" ::: "memory");
      sleep_until(t0, T_CHANGE);
      if (lane == 0) __builtin_amdgcn_raw_buffer_store_b32(a0 + 1u, rw, WS_A * 4, 0, AUX_SC1);
    } else {
      sleep_until(t0, T_LOOK);
#pragma unroll 1
      for (unsigned spins = 0; spins < SPIN_CAP; ++spins) {
        asm volatile("" ::: "memory");
        if ((unsigned)__builtin_amdgcn_readfirstlane(__builtin_amdgcn_raw_buffer_load_b32(rw, WS_A * 4, 0, AUX_SC1 | AUX_NT)) != a0) break;
        __builtin_amdgcn_s_sleep(64);
      }
    }
#pragma unroll 1
    for (int c = NCK1; c < NCK; ++c) recentre(c);
    asm volatile("s_waitcnt vmcnt(0) lgkmcnt(0)" ::: "memory");
    return;
  }

  const unsigned voff = ((lane & 31) * 16) | (key_bytes<M_HALF, 1>(lane >> 5) ^ key_bytes<M_WAVE, 2>(wave) ^
                                              key_bytes<M_WG, 2>(member) ^ key_bytes<M_HEAD, 6>(bh));
  auto load = [&](rsrc_t r, unsigned s, auto j) {
    return __builtin_amdgcn_raw_buffer_load_b128(r, voff ^ s ^ key_bytes<M_LOAD, 3>(j), bh * HEAD_BYTES, AUX_NT);
  };
  const u32x4 qv = __builtin_nontemporal_load(q + bh * 32 + (lane & 31));
  u32x4 T[RING];
  static_for<0, RING>([&](auto j) { T[j] = load(rsrc(k, TENSOR_BYTES), 0u, j); });
  fence();
  asm volatile("" : "+s"(t0));
  const unsigned ref0 = (unsigned)t0 + T_FIRST;
  unsigned* progress = old + 3 * MB + 4 + wave;
  const int c = lane & 15;
  float* sc = lds + wave * NIT * RING * 2;
  int level = 16;

  float m = NEG_BIG;
  {
    float* w = sc + (lane >> 5) * 4 + (c >> 2);
#pragma unroll 1
    for (int it = 0; it < NIT; ++it, w += RING * 2) {
      const unsigned long long now = __builtin_amdgcn_s_memrealtime();
      const unsigned ref = old[3 * MB + 1];
      const bool last = it == NIT - 1;
      const rsrc_t r = rsrc(last ? v : k, TENSOR_BYTES);
      const unsigned s = key_bytes<M_IT, 6>(it + 1);
      level = last ? LEVEL_MAX : level;
      float x[4];
      fence();
      static_for<0, RING>([&](auto jc) {
        constexpr int J = jc;
        float d = 0.f;
#pragma unroll
        for (int i = 0; i < 4; ++i) d = dot2(T[J][i], qv[i], d);
        x[J % 4] = d;
        asm volatile("" : "+v"(x[J % 4]));
        throttle<J>(level);
        T[J] = load(r, s, jc);
        fence();
        if constexpr (J % 4 == 3) {
          const float sv = reduce4(x, c);
          m = fmaxf(m, sv);
          w[(J / 4) * 8] = sv;
          fence();
        }
      });
      level = level_at<GAIN_K>(now, it, ref0, ref, progress);
    }
  }
  m = wave_max(m);
  fence();

  float l = 0.f;
  f32x2 acc[4] = {{0.f, 0.f}, {0.f, 0.f}, {0.f, 0.f}, {0.f, 0.f}};
  {
    const f32x4* pr = reinterpret_cast<const f32x4*>(sc) + (lane >> 5);
    f32x4 s0 = pr[0], s1;
#pragma unroll 1
    for (int it = 0; it < NIT; ++it) {
      unsigned long long now;
      unsigned ref;
      const bool last = it == NIT - 1;
      const rsrc_t r = rsrc(v, last ? 0u : TENSOR_BYTES);
      const unsigned s = key_bytes<M_IT, 6>(it + 1);
      level = last ? LEVEL_MAX : level;
      fence();
      static_for<0, RING>([&](auto jc) {
        constexpr int J = jc;
        if constexpr (J == 1) { now = __builtin_amdgcn_s_memrealtime(); s1 = pr[2]; ref = old[3 * MB + 1]; }
        if constexpr (J == 4) { pr += 4; s0 = pr[0]; }
        const float p = __builtin_amdgcn_exp2f((J < 4 ? s0[J % 4] : s1[J % 4]) - m);
        l += p;
        const f32x2 pp = {p, p};
#pragma unroll
        for (int i = 0; i < 4; ++i) {
          const unsigned w = T[J][i];
          acc[i] = __builtin_elementwise_fma(pp, f32x2{__uint_as_float(w << 16), __uint_as_float(w & 0xffff0000u)}, acc[i]);
        }
        asm volatile("" : "+v"(acc[0]), "+v"(acc[1]), "+v"(acc[2]), "+v"(acc[3]));
        throttle<J>(level);
        T[J] = load(r, s, jc);
        fence();
      });
      level = level_at<GAIN_V>(now, NIT + it, ref0, ref, progress);
    }
  }

  float* mrg = lds + LDS_MRG;
  {
    f32x4 h;
#pragma unroll
    for (int i = 0; i < 4; ++i) {
      const f32x2 t = swap32(acc[i / 2][i % 2], acc[2 + i / 2][i % 2]);
      h[i] = t[0] + t[1];
    }
    const f32x2 l2 = swap32(l, l);
    float* rec = mrg + wave * REC;
    *reinterpret_cast<f32x4*>(rec + 4 + 8 * (lane & 31) + 4 * (lane >> 5)) = h;
    *reinterpret_cast<f32x2*>(rec) = f32x2{m, l2[0] + l2[1]};
  }
  asm volatile("s_waitcnt lgkmcnt(0)" ::: "memory");
  __builtin_amdgcn_s_barrier();
  asm volatile("" ::: "memory");
  if (wave != 0) return;
  f32x2 ml[WAVES];
  f32x4 ow[WAVES];
  u32x4 so[3];
  u32x2 sm[3];
#pragma unroll
  for (int w = 0; w < WAVES; ++w) {
    ml[w] = *reinterpret_cast<const f32x2*>(mrg + w * REC);
    ow[w] = *reinterpret_cast<const f32x4*>(mrg + w * REC + 4 + 4 * lane);
  }
#pragma unroll
  for (int j = 0; j < 3; ++j) {
    const unsigned* od = old + (j == 0 && member != 0 ? member - 1 : j) * MB;
    so[j] = *reinterpret_cast<const u32x4*>(od + 4 * lane);
    sm[j] = *reinterpret_cast<const u32x2*>(od + D);
  }
  unsigned a = old[3 * MB];
  fence();
  float M = fmaxf(fmaxf(ml[0][0], ml[1][0]), fmaxf(ml[2][0], ml[3][0]));
  M = __uint_as_float(__float_as_uint(M) & ~1u);
  float L = 0.f;
  f32x4 o = {0.f, 0.f, 0.f, 0.f};
#pragma unroll
  for (int w = 0; w < WAVES; ++w) {
    const float wt = __builtin_amdgcn_exp2f(ml[w][0] - M);
    L = fmaf(wt, ml[w][1], L);
    o += wt * ow[w];
  }
  if (member != 0) {
    const u32x4 wo = (__builtin_bit_cast(u32x4, o) & ~1u) | (~so[0] & 1u);
    const u32x2 wm = (u32x2{__float_as_uint(M), __float_as_uint(L)} & ~1u) | (~sm[0] & 1u);
    __builtin_amdgcn_raw_buffer_store_b128(wo, rw, lane * 16, (member - 1) * MB * 4, AUX_SC1);
    __builtin_amdgcn_raw_buffer_store_b64(wm, rw, D * 4 + lane * (WS_HEAD * 4), (member - 1) * MB * 4, AUX_SC1);
    return;
  }

  u32x4 uo[3];
  u32x2 um[3];
#pragma unroll 1
  for (unsigned spins = 1; spins < SPIN_CAP; ++spins) {
#pragma unroll
    for (int j = 0; j < 3; ++j) {
      uo[j] = __builtin_amdgcn_raw_buffer_load_b128(rw, lane * 16, j * MB * 4, AUX_SC1 | AUX_NT);
      um[j] = __builtin_amdgcn_raw_buffer_load_b64(rw, D * 4, j * MB * 4, AUX_SC1 | AUX_NT);
    }
    fence();
    unsigned ok = 1u;
#pragma unroll
    for (int j = 0; j < 3; ++j) {
      const u32x4 x4 = uo[j] ^ so[j];
      const u32x2 x2 = um[j] ^ sm[j];
      ok &= x4[0] & x4[1] & x4[2] & x4[3] & x2[0] & x2[1];
    }
    if (__all(ok & 1u)) break;
    if ((spins & 63u) == 0u && lane == 0)
      __builtin_amdgcn_raw_buffer_store_b32(++a, rw, WS_A * 4, 0, AUX_SC1);
  }
  float MM = M;
#pragma unroll
  for (int j = 0; j < 3; ++j) MM = fmaxf(MM, __uint_as_float(um[j][0] & ~1u));
  const float w0 = __builtin_amdgcn_exp2f(M - MM);
  L *= w0;
  o *= w0;
#pragma unroll
  for (int j = 0; j < 3; ++j) {
    const float wt = __builtin_amdgcn_exp2f(__uint_as_float(um[j][0] & ~1u) - MM);
    L = fmaf(wt, __uint_as_float(um[j][1] & ~1u), L);
    o += wt * __builtin_bit_cast(f32x4, uo[j] & ~1u);
  }
  out[bh * 64 + lane] = __builtin_convertvector(o * (1.f / L), bf16x4);
}

extern "C" void kernel_launch(void* const* d_in, const int* in_sizes, int n_in, void* d_out, int out_size, void* d_ws,
                              size_t ws_size, hipStream_t stream) {
  attn_decode<<<BH * NSPLIT, (WAVES + 1) * 64, 0, stream>>>((const u32x4*)d_in[0], d_in[1], d_in[2], (unsigned*)d_ws, (bf16x4*)d_out);
}
```

```cpp
#include <hip/hip_runtime.h>
#include <hip/hip_bf16.h>
#include <stdint.h>
#include <type_traits>

constexpr int BH = 64, SKV = 16384, D = 256;
constexpr int NSPLIT = 4, WAVES = 4;
constexpr unsigned HEAD_BYTES = SKV * D * 2, TENSOR_BYTES = BH * HEAD_BYTES;
constexpr int RING = 8;
constexpr int NIT = SKV / (NSPLIT * WAVES * RING * 2);
constexpr unsigned MASK[20] = {0x0090, 0x2ff3, 0x22e4, 0x0011, 0x1000, 0x0051, 0x3473, 0x3560, 0x0f3d, 0x0666,
                               0x0bcf, 0x32b1, 0x16c4, 0x24ad, 0x34f0, 0x3087, 0x1127, 0x0c46, 0x2949, 0x37a0};
constexpr int M_HALF = 0, M_WAVE = 1, M_LOAD = 3, M_IT = 6, M_WG = 12, M_HEAD = 14;
template <int FIRST, int N> __host__ __device__ constexpr unsigned key_bytes(unsigned x) {
  unsigned r = 0;
  for (int b = 0; b < N; ++b) r ^= (0u - ((x >> b) & 1u)) & (MASK[FIRST + b] * (D * 2));
  return r;
}
constexpr bool masks_invertible() {
  unsigned m[14] = {};
  for (int i = 0; i < 14; ++i) m[i] = MASK[i];
  for (int c = 0; c < 14; ++c) {
    int p = c;
    while (p < 14 && !((m[p] >> c) & 1u)) ++p;
    if (p == 14) return false;
    const unsigned t = m[p]; m[p] = m[c]; m[c] = t;
    for (int i = c + 1; i < 14; ++i) if ((m[i] >> c) & 1u) m[i] ^= t;
  }
  return true;
}
static_assert(masks_invertible() && SKV == 1 << 14, "every key exactly once");
constexpr float C2 = 0.0625f * 1.4426950408889634f;
constexpr float NEG_BIG = -3.4028234663852886e38f;
constexpr int T_ALL = 13590, T_FIRST = 70, LEVEL0 = 11, LEVEL_MAX = 24, T_PUSH = 35, GAIN_K = 8, GAIN_V = 12;
constexpr int NCK = 4, NCK1 = 2;
constexpr unsigned T_HINT0 = 400, T_HINT1 = 2200, T_HINT_STEP = 3200, T_HINT_READ = 250;
constexpr unsigned T_NOTE = 7200, T_CHANGE = 8000, T_LOOK = 8400;
constexpr int MB = 288, WS_A = 3 * MB, WS_HEAD = WS_A + 32, WS_HINT = BH * WS_HEAD;
constexpr unsigned SPIN_CAP = 1u << 18;
constexpr int AUX_NT = 2, AUX_SC1 = 16;
constexpr int REC = D + 4, LDS_MRG = WAVES * NIT * RING * 2, LDS_OLD = LDS_MRG + WAVES * REC, LDS_FLOATS = LDS_OLD + 3 * MB + 8;

using u32x2 = __attribute__((ext_vector_type(2))) unsigned;
using u32x4 = __attribute__((ext_vector_type(4))) unsigned;
using f32x2 = __attribute__((ext_vector_type(2))) float;
using f32x4 = __attribute__((ext_vector_type(4))) float;
using bf16x2 = __attribute__((ext_vector_type(2))) __bf16;
using bf16x4 = __attribute__((ext_vector_type(4))) __bf16;
using rsrc_t = __amdgpu_buffer_rsrc_t;

__device__ __forceinline__ rsrc_t rsrc(const void* p, unsigned bytes) {
  return __builtin_amdgcn_make_buffer_rsrc((void*)p, 0, (int)bytes, 0x00020000);
}
__device__ __forceinline__ float dot2(unsigned k, unsigned q, float acc) {
  return __builtin_amdgcn_fdot2_f32_bf16(__builtin_bit_cast(bf16x2, k), __builtin_bit_cast(bf16x2, q), acc, false);
}
template <int CTRL> __device__ __forceinline__ float dpp(float x) {
  return __builtin_bit_cast(float, __builtin_amdgcn_mov_dpp(__builtin_bit_cast(int, x), CTRL, 0xf, 0xf, true));
}
constexpr int XOR1 = 0xB1, XOR2 = 0x4E, XOR7 = 0x141, XOR8 = 0x128;
__device__ __forceinline__ f32x2 swap16(float x) {
  auto s = __builtin_amdgcn_permlane16_swap(__float_as_uint(x), __float_as_uint(x), false, false);
  return f32x2{__uint_as_float(s[0]), __uint_as_float(s[1])};
}
__device__ __forceinline__ f32x2 swap32(float x, float y) {
  auto s = __builtin_amdgcn_permlane32_swap(__float_as_uint(x), __float_as_uint(y), false, false);
  return f32x2{__uint_as_float(s[0]), __uint_as_float(s[1])};
}
__device__ __forceinline__ float wave_max(float x) {
  x = fmaxf(x, dpp<XOR1>(x)); x = fmaxf(x, dpp<XOR2>(x)); x = fmaxf(x, dpp<XOR7>(x)); x = fmaxf(x, dpp<XOR8>(x));
  f32x2 s = swap16(x);
  x = fmaxf(s[0], s[1]);
  s = swap32(x, x);
  return fmaxf(s[0], s[1]);
}
__device__ __forceinline__ void sleep_until(unsigned long long t0, unsigned ticks) {
  while ((unsigned)(__builtin_amdgcn_s_memrealtime() - t0) < ticks) __builtin_amdgcn_s_sleep(8);
}
__device__ __forceinline__ void fence() { asm volatile("" ::: "memory"); __builtin_amdgcn_sched_barrier(0); }
template <int I, int N, typename F> __device__ __forceinline__ void static_for(F&& f) {
  if constexpr (I < N) { f(std::integral_constant<int, I>{}); static_for<I + 1, N>(f); }
}

template <int J> __device__ __forceinline__ void throttle(int level) {
  constexpr int JR = ((J & 1) << 2) | (J & 2) | ((J >> 2) & 1);
  asm volatile("s_waitcnt vmcnt(6)\n\ts_cmp_gt_i32 %0, %1\n\ts_cbranch_scc1 1f\n\t"
               "s_waitcnt vmcnt(5)\n\ts_cmp_gt_i32 %0, %2\n\ts_cbranch_scc1 1f\n\t"
               "s_waitcnt vmcnt(4)\n\ts_cmp_gt_i32 %0, %3\n\ts_cbranch_scc1 1f\n\t"
               "s_waitcnt vmcnt(3)\n1:" ::"s"(level), "n"(16 + JR), "n"(8 + JR), "n"(JR) : "scc");
}
template <int GAIN> __device__ __forceinline__ int level_at(unsigned long long now, int it, unsigned ref0, unsigned ref, unsigned* progress) {
  asm volatile("" : "+s"(now));
  const unsigned p = (unsigned)now - ((it * T_ALL) >> 7);
  *progress = p;
  const unsigned r = __builtin_amdgcn_readfirstlane(ref);
  const int lag = (int)(p - (it >= 8 && r - ref0 + 2048u < 4096u ? r : ref0));
  return LEVEL0 + ((lag * GAIN) >> 8);
}

__device__ __forceinline__ float reduce4(const float (&x)[4], int c) {
  const float a0 = x[0] + dpp<XOR8>(x[0]), a2 = x[2] + dpp<XOR8>(x[2]), a1 = x[1] + dpp<XOR8>(x[1]), a3 = x[3] + dpp<XOR8>(x[3]);
  const float y0 = (c & 8) ? a2 : a0, y1 = (c & 8) ? a3 : a1;
  const float b0 = y0 + dpp<XOR7>(y0), b1 = y1 + dpp<XOR7>(y1);
  float s = (c & 4) ? b1 : b0;
  s += dpp<XOR2>(s);
  s += dpp<XOR1>(s);
  const f32x2 t = swap16(s);
  return (t[0] + t[1]) * C2;
}

__global__ void __launch_bounds__((WAVES + 1) * 64, 2)
attn_decode(const void* q, const void* k, const void* v, unsigned* ws, bf16x4* __restrict__ out) {
  __shared__ __attribute__((aligned(16))) float lds[LDS_FLOATS];
  unsigned long long t0 = __builtin_amdgcn_s_memrealtime();
  const int bh = blockIdx.x / NSPLIT, member = blockIdx.x % NSPLIT;
  const int lane = threadIdx.x & 63, wave = __builtin_amdgcn_readfirstlane(threadIdx.x >> 6);
  const unsigned voff = ((lane & 31) * 16) | (key_bytes<M_HALF, 1>(lane >> 5) ^ key_bytes<M_WAVE, 2>(wave) ^
                                              key_bytes<M_WG, 2>(member) ^ key_bytes<M_HEAD, 6>(bh));
  unsigned vfirst[RING], qoff = bh * (D * 2) + (lane & 31) * 16;
  static_for<0, RING>([&](auto j) { vfirst[j] = voff ^ key_bytes<M_LOAD, 3>(j); asm volatile("" : "+v"(vfirst[j])); });
  asm volatile("" : "+v"(qoff));
  fence();
  asm volatile("" ::"s"(q), "s"(k), "s"(v), "s"(ws), "s"(out));
  unsigned* old = reinterpret_cast<unsigned*>(lds + LDS_OLD);
  const rsrc_t rw = rsrc(ws + bh * WS_HEAD, WS_HEAD * 4);

  if (wave == WAVES) {
    __builtin_amdgcn_s_setprio(3);
    old[3 * MB + 1] = (unsigned)t0 + T_FIRST;
    const rsrc_t rh = rsrc(ws + WS_HINT, NCK * 8 * 4);
    sleep_until(t0, T_HINT0 + blockIdx.x);
    const unsigned h0 = __builtin_amdgcn_raw_buffer_load_b32(rh, lane * 4, 0, AUX_SC1);
    auto recentre = [&](int c) {
      sleep_until(t0, T_HINT1 + c * T_HINT_STEP);
      if (blockIdx.x < 8) {
        asm volatile("" ::: "memory");
        const u32x4 p = *reinterpret_cast<const u32x4*>(old + 3 * MB + 4);
        const unsigned mean = p[0] + (unsigned)((int)(p[1] + p[2] + p[3] - 3u * p[0]) >> 2);
        if (lane == c * 8 + blockIdx.x) __builtin_amdgcn_raw_buffer_store_b32((mean & ~1u) | (~h0 & 1u), rh, lane * 4, 0, AUX_SC1);
      }
      sleep_until(t0, T_HINT1 + c * T_HINT_STEP + T_HINT_READ + blockIdx.x);
      asm volatile("" ::: "memory");
      const unsigned h = __builtin_amdgcn_raw_buffer_load_b32(rh, lane * 4, 0, AUX_SC1);
      if (((__ballot((h ^ h0) & 1u) >> (c * 8)) & 0xffu) == 0xffu) {
        unsigned sum = 0;
#pragma unroll
        for (int b = 0; b < 8; ++b) sum += __builtin_amdgcn_readlane(h, c * 8 + b) - (unsigned)t0;
        old[3 * MB + 1] = (unsigned)t0 + (unsigned)max(((int)sum >> 3) - T_PUSH, T_FIRST);
      }
    };
#pragma unroll 1
    for (int c = 0; c < NCK1; ++c) recentre(c);
    sleep_until(t0, T_NOTE);
    const unsigned a0 = __builtin_amdgcn_readfirstlane(__builtin_amdgcn_raw_buffer_load_b32(rw, WS_A * 4, 0, AUX_SC1 | AUX_NT));
    const int j0 = member == 0 ? 0 : member - 1, j1 = member == 0 ? 3 : member;
    for (int j = j0; j < j1; ++j) {
      const u32x4 o4 = __builtin_amdgcn_raw_buffer_load_b128(rw, lane * 16, j * MB * 4, AUX_SC1 | AUX_NT);
      const u32x2 ml = __builtin_amdgcn_raw_buffer_load_b64(rw, D * 4, j * MB * 4, AUX_SC1 | AUX_NT);
      *reinterpret_cast<u32x4*>(old + j * MB + lane * 4) = o4;
      *reinterpret_cast<u32x2*>(old + j * MB + D) = ml;
    }
    if (member == 0) {
      old[3 * MB] = a0 + 1u;
      asm volatile("s_waitcnt vmcnt(0) lgkmcnt(0)" ::: "memory");
      sleep_until(t0, T_CHANGE);
      if (lane == 0) __builtin_amdgcn_raw_buffer_store_b32(a0 + 1u, rw, WS_A * 4, 0, AUX_SC1);
    } else {
      sleep_until(t0, T_LOOK);
#pragma unroll 1
      for (unsigned spins = 0; spins < SPIN_CAP; ++spins) {
        asm volatile("" ::: "memory");
        if ((unsigned)__builtin_amdgcn_readfirstlane(__builtin_amdgcn_raw_buffer_load_b32(rw, WS_A * 4, 0, AUX_SC1 | AUX_NT)) != a0) break;
        __builtin_amdgcn_s_sleep(64);
      }
    }
#pragma unroll 1
    for (int c = NCK1; c < NCK; ++c) recentre(c);
    asm volatile("s_waitcnt vmcnt(0) lgkmcnt(0)" ::: "memory");
    return;
  }

  auto load = [&](rsrc_t r, unsigned s, auto j) {
    return __builtin_amdgcn_raw_buffer_load_b128(r, voff ^ s ^ key_bytes<M_LOAD, 3>(j), bh * HEAD_BYTES, AUX_NT);
  };
  const u32x4 qv = __builtin_amdgcn_raw_buffer_load_b128(rsrc(q, BH * D * 2), qoff, 0, AUX_NT);
  fence();
  u32x4 T[RING];
  static_for<0, RING>([&](auto j) {
    T[j] = __builtin_amdgcn_raw_buffer_load_b128(rsrc(k, TENSOR_BYTES), vfirst[j], bh * HEAD_BYTES, AUX_NT);
    fence();
  });
  asm volatile("" : "+s"(t0));
  const unsigned ref0 = (unsigned)t0 + T_FIRST;
  unsigned* progress = old + 3 * MB + 4 + wave;
  const int c = lane & 15;
  float* sc = lds + wave * NIT * RING * 2;
  int level = 16;

  float m = NEG_BIG;
  {
    float* w = sc + (lane >> 5) * 4 + (c >> 2);
#pragma unroll 1
    for (int it = 0; it < NIT; ++it, w += RING * 2) {
      const unsigned long long now = __builtin_amdgcn_s_memrealtime();
      const unsigned ref = old[3 * MB + 1];
      const bool last = it == NIT - 1;
      const rsrc_t r = rsrc(last ? v : k, TENSOR_BYTES);
      const unsigned s = key_bytes<M_IT, 6>(it + 1);
      level = last ? LEVEL_MAX : level;
      float x[4];
      fence();
      static_for<0, RING>([&](auto jc) {
        constexpr int J = jc;
        float d = 0.f;
#pragma unroll
        for (int i = 0; i < 4; ++i) d = dot2(T[J][i], qv[i], d);
        x[J % 4] = d;
        asm volatile("" : "+v"(x[J % 4]));
        throttle<J>(level);
        T[J] = load(r, s, jc);
        fence();
        if constexpr (J % 4 == 3) {
          const float sv = reduce4(x, c);
          m = fmaxf(m, sv);
          w[(J / 4) * 8] = sv;
          fence();
        }
      });
      level = level_at<GAIN_K>(now, it, ref0, ref, progress);
    }
  }
  m = wave_max(m);
  fence();

  float l = 0.f;
  f32x2 acc[4] = {{0.f, 0.f}, {0.f, 0.f}, {0.f, 0.f}, {0.f, 0.f}};
  {
    const f32x4* pr = reinterpret_cast<const f32x4*>(sc) + (lane >> 5);
    f32x4 s0 = pr[0], s1;
#pragma unroll 1
    for (int it = 0; it < NIT; ++it) {
      unsigned long long now;
      unsigned ref;
      const bool last = it == NIT - 1;
      const rsrc_t r = rsrc(v, last ? 0u : TENSOR_BYTES);
      const unsigned s = key_bytes<M_IT, 6>(it + 1);
      level = last ? LEVEL_MAX : level;
      fence();
      static_for<0, RING>([&](auto jc) {
        constexpr int J = jc;
        if constexpr (J == 1) { now = __builtin_amdgcn_s_memrealtime(); s1 = pr[2]; ref = old[3 * MB + 1]; }
        if constexpr (J == 4) { pr += 4; s0 = pr[0]; }
        const float p = __builtin_amdgcn_exp2f((J < 4 ? s0[J % 4] : s1[J % 4]) - m);
        l += p;
        const f32x2 pp = {p, p};
#pragma unroll
        for (int i = 0; i < 4; ++i) {
          const unsigned w = T[J][i];
          acc[i] = __builtin_elementwise_fma(pp, f32x2{__uint_as_float(w << 16), __uint_as_float(w & 0xffff0000u)}, acc[i]);
        }
        asm volatile("" : "+v"(acc[0]), "+v"(acc[1]), "+v"(acc[2]), "+v"(acc[3]));
        throttle<J>(level);
        T[J] = load(r, s, jc);
        fence();
      });
      level = level_at<GAIN_V>(now, NIT + it, ref0, ref, progress);
    }
  }

  float* mrg = lds + LDS_MRG;
  {
    f32x4 h;
#pragma unroll
    for (int i = 0; i < 4; ++i) {
      const f32x2 t = swap32(acc[i / 2][i % 2], acc[2 + i / 2][i % 2]);
      h[i] = t[0] + t[1];
    }
    const f32x2 l2 = swap32(l, l);
    float* rec = mrg + wave * REC;
    *reinterpret_cast<f32x4*>(rec + 4 + 8 * (lane & 31) + 4 * (lane >> 5)) = h;
    *reinterpret_cast<f32x2*>(rec) = f32x2{m, l2[0] + l2[1]};
  }
  asm volatile("s_waitcnt lgkmcnt(0)" ::: "memory");
  __builtin_amdgcn_s_barrier();
  asm volatile("" ::: "memory");
  if (wave != 0) return;
  f32x2 ml[WAVES];
  f32x4 ow[WAVES];
  u32x4 so[3];
  u32x2 sm[3];
#pragma unroll
  for (int w = 0; w < WAVES; ++w) {
    ml[w] = *reinterpret_cast<const f32x2*>(mrg + w * REC);
    ow[w] = *reinterpret_cast<const f32x4*>(mrg + w * REC + 4 + 4 * lane);
  }
#pragma unroll
  for (int j = 0; j < 3; ++j) {
    const unsigned* od = old + (j == 0 && member != 0 ? member - 1 : j) * MB;
    so[j] = *reinterpret_cast<const u32x4*>(od + 4 * lane);
    sm[j] = *reinterpret_cast<const u32x2*>(od + D);
  }
  unsigned a = old[3 * MB];
  fence();
  float M = fmaxf(fmaxf(ml[0][0], ml[1][0]), fmaxf(ml[2][0], ml[3][0]));
  M = __uint_as_float(__float_as_uint(M) & ~1u);
  float L = 0.f;
  f32x4 o = {0.f, 0.f, 0.f, 0.f};
#pragma unroll
  for (int w = 0; w < WAVES; ++w) {
    const float wt = __builtin_amdgcn_exp2f(ml[w][0] - M);
    L = fmaf(wt, ml[w][1], L);
    o += wt * ow[w];
  }
  if (member != 0) {
    const u32x4 wo = (__builtin_bit_cast(u32x4, o) & ~1u) | (~so[0] & 1u);
    const u32x2 wm = (u32x2{__float_as_uint(M), __float_as_uint(L)} & ~1u) | (~sm[0] & 1u);
    __builtin_amdgcn_raw_buffer_store_b128(wo, rw, lane * 16, (member - 1) * MB * 4, AUX_SC1);
    __builtin_amdgcn_raw_buffer_store_b64(wm, rw, D * 4 + lane * (WS_HEAD * 4), (member - 1) * MB * 4, AUX_SC1);
    return;
  }

  u32x4 uo[3];
  u32x2 um[3];
#pragma unroll 1
  for (unsigned spins = 1; spins < SPIN_CAP; ++spins) {
#pragma unroll
    for (int j = 0; j < 3; ++j) {
      uo[j] = __builtin_amdgcn_raw_buffer_load_b128(rw, lane * 16, j * MB * 4, AUX_SC1 | AUX_NT);
      um[j] = __builtin_amdgcn_raw_buffer_load_b64(rw, D * 4, j * MB * 4, AUX_SC1 | AUX_NT);
    }
    fence();
    unsigned ok = 1u;
#pragma unroll
    for (int j = 0; j < 3; ++j) {
      const u32x4 x4 = uo[j] ^ so[j];
      const u32x2 x2 = um[j] ^ sm[j];
      ok &= x4[0] & x4[1] & x4[2] & x4[3] & x2[0] & x2[1];
    }
    if (__all(ok & 1u)) break;
    if ((spins & 63u) == 0u && lane == 0)
      __builtin_amdgcn_raw_buffer_store_b32(++a, rw, WS_A * 4, 0, AUX_SC1);
  }
  float MM = M;
#pragma unroll
  for (int j = 0; j < 3; ++j) MM = fmaxf(MM, __uint_as_float(um[j][0] & ~1u));
  const float w0 = __builtin_amdgcn_exp2f(M - MM);
  L *= w0;
  o *= w0;
#pragma unroll
  for (int j = 0; j < 3; ++j) {
    const float wt = __builtin_amdgcn_exp2f(__uint_as_float(um[j][0] & ~1u) - MM);
    L = fmaf(wt, __uint_as_float(um[j][1] & ~1u), L);
    o += wt * __builtin_bit_cast(f32x4, uo[j] & ~1u);
  }
  out[bh * 64 + lane] = __builtin_convertvector(o * (1.f / L), bf16x4);
}

extern "C" void kernel_launch(void* const* d_in, const int* in_sizes, int n_in, void* d_out, int out_size, void* d_ws,
                              size_t ws_size, hipStream_t stream) {
  attn_decode<<<BH * NSPLIT, (WAVES + 1) * 64, 0, stream>>>(d_in[0], d_in[1], d_in[2], (unsigned*)d_ws, (bf16x4*)d_out);
}
```
